```python
import torch

def _softmax(x, axis=-1):
    return torch.softmax(x, dim=axis)
def _gelu(x):
    return torch.nn.functional.gelu(x, approximate="tanh")
def _silu(x):
    return torch.nn.functional.silu(x)
def _sigmoid(x):
    return torch.sigmoid(x)
def _layernorm(x, axis=-1, eps=1e-5):
    m = x.mean(dim=axis, keepdim=True)
    v = x.var(dim=axis, keepdim=True, unbiased=False)
    return (x - m) / torch.sqrt(v + eps)
def _scatter_add(tgt, idx, src, axis=0):
    return tgt.clone().index_add_(axis, idx, src)
def _segment_sum(src, seg, n):
    out = torch.zeros((n,) + src.shape[1:], dtype=src.dtype)
    return out.index_add_(0, seg, src)
def _pad(x, pw):
    flat = tuple(v for pair in reversed(pw) for v in pair)
    return torch.nn.functional.pad(x, flat)

kernel_name = 'gated_mix__sliding_block__mixer_channel_mlp__prompt'

def setup_inputs():

    _w = (lambda a: (lambda t: t.to(torch.bfloat16) if t.is_floating_point() else t)(torch.as_tensor(a)))
    def _A_setup():
        B, S, D = 1, 2048, 576
        return {
            "x": _w((lambda *s: torch.randn(*s, dtype=torch.bfloat16) * 0.05)(B, S, D)),
            "wqkv": _w((lambda *s: torch.randn(*s, dtype=torch.bfloat16) * 0.8)(D, 3 * D)),
            "wo": _w((lambda *s: torch.randn(*s, dtype=torch.bfloat16) * 0.05)(D, D)),
        }
    _A_in = _A_setup()

    def _B_setup():
        B, S, D, F = 1, 2048, 576, 4608
        return {
            "x": _w((lambda *s: torch.randn(*s, dtype=torch.bfloat16) * 0.05)(B, S, D)),
            "w1": _w((lambda *s: torch.randn(*s, dtype=torch.bfloat16) * 3.2)(D, F) * (D ** -0.5)),
            "b1": _w((lambda *s: torch.randn(*s, dtype=torch.bfloat16) * 1.6)(F)),
            "w2": _w((lambda *s: torch.randn(*s, dtype=torch.bfloat16) * 0.1)(F, D) * (F ** -0.5)),
            "b2": _w((lambda *s: torch.randn(*s, dtype=torch.bfloat16) * 0.2)(D)),
        }
    _B_in = _B_setup()

    return {
        "A_x": _A_in["x"],
        "A_wqkv": _A_in["wqkv"],
        "A_wo": _A_in["wo"],
        "B_x": _B_in["x"],
        "B_w1": _B_in["w1"],
        "B_b1": _B_in["b1"],
        "B_w2": _B_in["w2"],
        "B_b2": _B_in["b2"],
        "gate_w": _w((lambda *s: torch.randn(*s, dtype=torch.bfloat16) * 0.05)(576) * 16.0),
    }

def reference(A_x, A_wqkv, A_wo, B_x, B_w1, B_b1, B_w2, B_b2, gate_w):
    def _A_ref(x, wqkv, wo):
        B, S, D = x.shape
        H = 32; Dh = D // H; W = 256
        qkv = (lambda x, *d: x.permute(*d))((x @ wqkv).reshape(B, S, 3, H, Dh), 2, 0, 3, 1, 4)
        q, k, v = qkv[0], qkv[1], qkv[2]
        scores = torch.einsum("bhqd,bhkd->bhqk", q, k) * (Dh ** -0.5)
        idx = torch.arange(S)
        mask = (idx[:, None] - idx[None, :] >= 0) & (idx[:, None] - idx[None, :] < W)
        scores = torch.where(mask[None, None], scores, -1e9)
        p = _softmax(scores, axis=-1)
        o = torch.einsum("bhqk,bhkd->bhqd", p, v)
        return (lambda x, *d: x.permute(*d))(o, 0, 2, 1, 3).reshape(B, S, D) @ wo

    def _B_ref(x, w1, b1, w2, b2):
        y = _layernorm(x, axis=-1)
        h = _gelu(torch.einsum("bsd,df->bsf", y, w1) + b1)
        return torch.einsum("bsf,fd->bsd", h, w2) + b2

    x = A_x
    g = _sigmoid((x * gate_w).sum(-1, keepdims=True))
    a = _A_ref(x, A_wqkv, A_wo)
    b = _B_ref(x, B_w1, B_b1, B_w2, B_b2)
    return g * a + (1 - g) * b
```

```cpp
#include <hip/hip_runtime.h>
#include <hip/hip_bf16.h>
typedef __hip_bfloat16 bf16;
constexpr int S=2048, D=576, F=4608, H=32, DH=18, W=256;
__device__ inline float b2f(bf16 x){ return __bfloat162float(x); }
__device__ inline bf16 f2b(float x){ return __float2bfloat16(x); }
__device__ inline float rb(float x){ return b2f(f2b(x)); }

__global__ void k_gemm(const bf16* A, const bf16* B, bf16* C, int M, int N, int K){
  int n = blockIdx.x*256+threadIdx.x; int m = blockIdx.y;
  if(n>=N) return;
  float acc=0; for(int k=0;k<K;k++) acc += b2f(A[(size_t)m*K+k])*b2f(B[(size_t)k*N+n]);
  C[(size_t)m*N+n]=f2b(acc);
}
__global__ void k_attn(const bf16* qkv, bf16* o){
  int h = blockIdx.x, q = blockIdx.y*64+threadIdx.x;
  const bf16* qp = qkv + (size_t)q*3*D + h*DH;
  float qv[DH]; for(int d=0;d<DH;d++) qv[d]=b2f(qp[d]);
  float m=-1e30f;
  int k0 = q-W+1<0?0:q-W+1;
  for(int k=k0;k<=q;k++){ float s=0; const bf16* kp=qkv+(size_t)k*3*D+D+h*DH; for(int d=0;d<DH;d++) s+=qv[d]*b2f(kp[d]); s=rb(rb(s)*rb(0.23570226f)); m=fmaxf(m,s);}
  float l=0; float acc[DH]; for(int d=0;d<DH;d++) acc[d]=0;
  for(int k=k0;k<=q;k++){ float s=0; const bf16* kp=qkv+(size_t)k*3*D+D+h*DH; for(int d=0;d<DH;d++) s+=qv[d]*b2f(kp[d]); s=rb(rb(s)*rb(0.23570226f)); float p=__expf(s-m); l+=p; const bf16* vp=qkv+(size_t)k*3*D+2*D+h*DH; for(int d=0;d<DH;d++) acc[d]+=p*b2f(vp[d]); }
  for(int d=0;d<DH;d++) o[(size_t)q*D+h*DH+d]=f2b(acc[d]/l);
}
__global__ void k_ln(const bf16* x, bf16* y){
  int m=blockIdx.x*64+threadIdx.x; float s=0; for(int d=0;d<D;d++) s+=b2f(x[(size_t)m*D+d]); s/=D;
  float v=0; for(int d=0;d<D;d++){ float t=b2f(x[(size_t)m*D+d])-s; v+=t*t;} v/=D;
  float r=rsqrtf(v+1e-5f);
  for(int d=0;d<D;d++) y[(size_t)m*D+d]=f2b((b2f(x[(size_t)m*D+d])-s)*r);
}
__global__ void k_gelu(bf16* h, const bf16* b1){
  size_t i=(size_t)blockIdx.x*256+threadIdx.x; int f=i%F; float x=b2f(h[i])+b2f(b1[f]);
  float u=0.7978845608f*(x+0.044715f*x*x*x); h[i]=f2b(0.5f*x*(1.f+tanhf(u)));
}
__global__ void k_final(const bf16* x, const bf16* gw, const bf16* a, const bf16* b, const bf16* b2, bf16* out){
  int m=blockIdx.x; __shared__ float g;
  if(threadIdx.x==0){ float s=0; for(int d=0;d<D;d++) s+=b2f(x[(size_t)m*D+d])*b2f(gw[d]); g=1.f/(1.f+__expf(-s)); }
  __syncthreads();
  for(int d=threadIdx.x; d<D; d+=blockDim.x){ float bb=b2f(b[(size_t)m*D+d])+b2f(b2[d]); out[(size_t)m*D+d]=f2b(g*b2f(a[(size_t)m*D+d])+(1.f-g)*bb); }
}
extern "C" void kernel_launch(void* const* d_in, const int* in_sizes, int n_in, void* d_out, int out_size, void* d_ws, size_t ws_size, hipStream_t stream){
  const bf16* Ax=(const bf16*)d_in[0]; const bf16* wqkv=(const bf16*)d_in[1]; const bf16* wo=(const bf16*)d_in[2];
  const bf16* Bx=(const bf16*)d_in[3]; const bf16* w1=(const bf16*)d_in[4]; const bf16* b1=(const bf16*)d_in[5];
  const bf16* w2=(const bf16*)d_in[6]; const bf16* b2=(const bf16*)d_in[7]; const bf16* gw=(const bf16*)d_in[8];
  bf16* ws=(bf16*)d_ws;
  bf16* qkv=ws; bf16* o=qkv+(size_t)S*3*D; bf16* a=o+(size_t)S*D; bf16* y=a+(size_t)S*D; bf16* h=y+(size_t)S*D; bf16* b=h+(size_t)S*F;
  k_gemm<<<dim3((3*D+255)/256,S),256,0,stream>>>(Ax,wqkv,qkv,S,3*D,D);
  k_attn<<<dim3(H,S/64),64,0,stream>>>(qkv,o);
  k_gemm<<<dim3((D+255)/256,S),256,0,stream>>>(o,wo,a,S,D,D);
  k_ln<<<S/64,64,0,stream>>>(Ax,y);
  k_gemm<<<dim3((F+255)/256,S),256,0,stream>>>(y,w1,h,S,F,D);
  k_gelu<<<(size_t)S*F/256,256,0,stream>>>(h,b1);
  k_gemm<<<dim3((D+255)/256,S),256,0,stream>>>(h,w2,b,S,D,F);
  k_final<<<S,192,0,stream>>>(Ax,gw,a,b,b2,(bf16*)d_out);
}
```

```cpp
#include <hip/hip_runtime.h>
#include <stdint.h>

typedef __attribute__((ext_vector_type(8))) __bf16 bf16x8;
typedef __attribute__((ext_vector_type(2))) __bf16 bf16x2;
typedef __attribute__((ext_vector_type(4))) short s16x4;
typedef __attribute__((ext_vector_type(16))) float f32x16;
typedef __attribute__((ext_vector_type(4))) float f32x4;
typedef __attribute__((ext_vector_type(4))) unsigned u32x4;
typedef __attribute__((ext_vector_type(2))) unsigned u32x2;
typedef __attribute__((ext_vector_type(4))) int i32x4;
typedef __amdgpu_buffer_rsrc_t rsrc_t;
typedef unsigned long long u64;
#define LAS __attribute__((address_space(3)))
#define GAS __attribute__((address_space(1)))
#define DI __device__ __forceinline__

constexpr int SEQ = 2048, DM = 576, FF = 4608;
constexpr size_t WS_EPOCH = 0;
constexpr size_t WS_KVFLAG = 4096;
constexpr size_t WS_K16 = 65536;
constexpr size_t WS_KL = WS_K16 + 32 * 64 * 1024;
constexpr size_t WS_VT = WS_KL + 32 * 64 * 128;
constexpr size_t WS_PART = WS_VT + 32 * 64 * 2048;
constexpr size_t WS_DBG = WS_PART + (size_t)16 * 16 * 18 * 4 * 2 * 1024;

constexpr unsigned L_QKV_STAGE = 32768;
constexpr unsigned L_VSCR = 32768;
constexpr unsigned L_H = 0;
constexpr unsigned L_G1 = 73728;
constexpr unsigned L_G2A = 73728, L_G2B = 0, L_G2C = 115712;
constexpr unsigned L_OX = 115712;
constexpr unsigned L_OL = L_OX + 8192;
constexpr unsigned L_B1 = 157696;
constexpr unsigned L_B2 = L_B1 + 1152;
constexpr unsigned L_END = L_B2 + 2304;
static_assert(L_END <= 163840, "lds");

DI unsigned pack2(float a, float b) { bf16x2 v; v[0] = (__bf16)a; v[1] = (__bf16)b; return __builtin_bit_cast(unsigned, v); }
DI float u2f(unsigned u) { return __builtin_bit_cast(float, u); }
DI unsigned f2u(float f) { return __builtin_bit_cast(unsigned, f); }
DI float bf_lo(unsigned u) { return u2f(u << 16); }
DI float bf_hi(unsigned u) { return u2f(u & 0xffff0000u); }
DI f32x16 mfma(u32x4 a, u32x4 b, f32x16 c) {
  return __builtin_amdgcn_mfma_f32_32x32x16_bf16(__builtin_bit_cast(bf16x8, a), __builtin_bit_cast(bf16x8, b), c, 0, 0, 0);
}
DI i32x4 make_srd(const void* p, unsigned bytes) {
  u64 a = (u64)p; i32x4 r;
  r[0] = __builtin_amdgcn_readfirstlane((int)(unsigned)a);
  r[1] = __builtin_amdgcn_readfirstlane((int)((unsigned)(a >> 32) & 0xffffu));
  r[2] = (int)bytes; r[3] = 0x00020000;
  return r;
}
DI rsrc_t mk_rsrc(const void* p, unsigned bytes) { return __builtin_amdgcn_make_buffer_rsrc((void*)p, 0, bytes, 0x00020000); }
DI void dma16(i32x4 srd, unsigned voff, unsigned soff, unsigned lds) {
  asm volatile("s_mov_b32 m0, %0\n\ts_nop 0\n\tbuffer_load_dwordx4 %1, %2, %3 offen lds" ::"s"(lds), "v"(voff), "s"(srd), "s"(soff));
}
#define WAIT_BAR(N) asm volatile("s_waitcnt vmcnt(" #N ") lgkmcnt(0)\n\ts_barrier" ::: "memory")
#define WB_QKV(N) WAIT_BAR(N)
#define WB_G1(N) WAIT_BAR(N)
#define WB_G2(N) WAIT_BAR(N)
#define SCHED_FENCE __builtin_amdgcn_sched_barrier(0)

__shared__ __attribute__((aligned(16))) char smem[163840];
DI u32x4 lds_rd128(unsigned a) { return *(LAS u32x4*)((LAS char*)smem + a); }
DI void lds_wr128(unsigned a, u32x4 v) { *(LAS u32x4*)((LAS char*)smem + a) = v; }
DI void lds_wr64(unsigned a, u32x2 v) { *(LAS u32x2*)((LAS char*)smem + a) = v; }
DI void lds_wr32(unsigned a, unsigned v) { *(LAS unsigned*)((LAS char*)smem + a) = v; }
DI unsigned lds_rd32(unsigned a) { return *(LAS unsigned*)((LAS char*)smem + a); }
DI f32x4 lds_rdf4(unsigned a) { return *(LAS f32x4*)((LAS char*)smem + a); }
DI u32x4 tr_frag(unsigned a0, unsigned a1) {
  s16x4 lo = __builtin_amdgcn_ds_read_tr16_b64_v4i16((LAS s16x4*)((LAS char*)smem + a0));
  s16x4 hi = __builtin_amdgcn_ds_read_tr16_b64_v4i16((LAS s16x4*)((LAS char*)smem + a1));
  return __builtin_bit_cast(u32x4, __builtin_shufflevector(lo, hi, 0, 1, 2, 3, 4, 5, 6, 7));
}
DI int fresh_lane() { int l; asm volatile("v_mbcnt_lo_u32_b32 %0, -1, 0\n\tv_mbcnt_hi_u32_b32 %0, -1, %0" : "=v"(l)); return l; }
#define LANEVARS const int lane = fresh_lane(); const int r = lane & 31, h = lane >> 5; const int blk = (lane >> 4) & 1, q4 = (lane & 15) >> 2, p4 = lane & 3; (void)r; (void)h; (void)blk; (void)q4; (void)p4;
DI float lo_bcast(float v) { unsigned x = f2u(v); auto p = __builtin_amdgcn_permlane32_swap(x, x, false, false); unsigned y = p[0]; return u2f(y); }
DI float hi_bcast(float v) { unsigned x = f2u(v); auto p = __builtin_amdgcn_permlane32_swap(x, x, false, false); unsigned y = p[1]; return u2f(y); }
DI float half_sum(float v) { unsigned x = f2u(v); auto p = __builtin_amdgcn_permlane32_swap(x, x, false, false); unsigned y0 = p[0], y1 = p[1]; return u2f(y0) + u2f(y1); }
DI float half_max(float v) { unsigned x = f2u(v); auto p = __builtin_amdgcn_permlane32_swap(x, x, false, false); unsigned y0 = p[0], y1 = p[1]; return fmaxf(u2f(y0), u2f(y1)); }

struct Params {
  const unsigned short* x; const unsigned short* wqkv; const unsigned short* wo;
  const unsigned short* w1; const unsigned short* b1; const unsigned short* w2; const unsigned short* b2; const unsigned short* gw;
  char* ws; unsigned short* out;
};

#define STAMP(k)

DI float gelu_scaled(float x, float omg) {
  float x2 = x * x;
  float t = x * __builtin_fmaf(x2, -0.1029432397f, -2.3022081986f);
  float e = __builtin_amdgcn_exp2f(t);
  float r = __builtin_amdgcn_rcpf(1.0f + e);
  return (x * omg) * r;
}

__global__ __launch_bounds__(512, 2) void fused_kernel(Params P) {
  const int w = __builtin_amdgcn_readfirstlane(threadIdx.x >> 6);
  const int t = w & 3, u = w >> 2;
  const int b = blockIdx.x, xcd = b & 7, slot = b >> 3;
  const int m = 8 * (xcd & 1) + (slot & 7), s = 4 * (xcd >> 1) + (slot >> 3);
  const int tok0 = 128 * m;
  const int head = 2 * s + u;
  STAMP(0);

  const i32x4 rs_w1 = make_srd(P.w1, DM * FF * 2);
  u64 ep1;
  u32x4 xf[36];
  u32x4 qf0; unsigned qf1;
  float gate, omg, ln_rstd, ln_nb;

  auto issue_g1k = [&](int i, int part, int k, unsigned vg1) {
    const int g = 3 * w + k;
    dma16(rs_w1, vg1, (192 * part + 8 * g) * 9216 + (288 * s + 64 * i) * 2, L_G1 + part * 24576 + g * 1024);
  };
  auto issue_g1 = [&](int i, int part, unsigned vg1) { issue_g1k(i, part, 0, vg1); issue_g1k(i, part, 1, vg1); issue_g1k(i, part, 2, vg1); };

  {
    LANEVARS
    const int tid = w * 64 + lane;
    const i32x4 rs_x = make_srd(P.x, SEQ * DM * 2);
    const i32x4 rs_wqkv = make_srd(P.wqkv, DM * 3 * DM * 2);
    unsigned gwa, gwb, b1v; u64 epoch;
    {
      const unsigned short* g0 = P.gw + 64 * w + lane;
      const unsigned short* g1 = P.gw + 512 + lane;
      const unsigned short* pb1 = P.b1 + 288 * s + (tid < 288 ? tid : 0);
      const char* pe = P.ws + WS_EPOCH;
      asm volatile("global_load_ushort %0, %1, off" : "=v"(gwa) : "v"(g0));
      asm volatile("global_load_ushort %0, %1, off" : "=v"(gwb) : "v"(g1));
      asm volatile("global_load_ushort %0, %1, off" : "=v"(b1v) : "v"(pb1));
      asm volatile("global_load_dwordx2 %0, %1, off sc1" : "=v"(epoch) : "v"(pe));
    }
    unsigned vx[2];
#pragma unroll
    for (int par = 0; par < 2; ++par) {
      int rowi = lane >> 3;
      int swz = (4 * par + (rowi >> 1)) & 7;
      vx[par] = rowi * 1152 + (((lane & 7) ^ swz) << 4);
    }
    unsigned vw; bool whole;
    {
      int q = lane >> 4, sg = ((lane & 15) - 4 * q) & 15;
      int mat = sg < 5 ? 0 : (sg < 11 ? 1 : 2);
      int ch = sg < 5 ? sg : (sg < 11 ? sg - 6 : sg - 11);
      whole = (sg == 5);
      vw = q * 3456 + mat * 1152 + ch * 16;
    }
    auto issue_qkvk = [&](int p, int k) {
      unsigned base = (p & 3) * L_QKV_STAGE;
      const int i = 2 * w + (k & 1);
      if (k < 2) dma16(rs_x, vx[k], (tok0 + 8 * i) * 1152 + p * 128, base + i * 1024);
      else {
        unsigned so = (64 * p + 4 * i) * 3456 + 72 * s, lb = base + 16384 + i * 1024;
        asm volatile("" : "+s"(so), "+s"(lb));
        if (!whole) dma16(rs_wqkv, vw, so, lb);
      }
    };
    auto issue_qkv = [&](int p) { issue_qkvk(p, 0); issue_qkvk(p, 1); issue_qkvk(p, 2); issue_qkvk(p, 3); };
    auto write_gx = [&](int p, unsigned gv) {
      unsigned a = (p & 3) * L_QKV_STAGE + 16384 + (lane >> 2) * 1024 + (lane & 3) * 256 + ((80 + 64 * (lane & 3)) & 255);
      u32x2 v; v[0] = (gv & 0xffffu) | 0x3F800000u; v[1] = 0;
      lds_wr64(a, v);
    };
    issue_qkv(0); issue_qkv(1); issue_qkv(2); issue_qkv(3);
    asm volatile("s_waitcnt vmcnt(16)" : "+v"(gwa), "+v"(gwb), "+v"(b1v), "+v"(epoch));
    if (w < 4) write_gx(w, gwa);
    if (tid < 288) lds_wr32(L_B1 + 4 * tid, b1v << 16);
    ep1 = ((u64)(unsigned)__builtin_amdgcn_readfirstlane((int)(unsigned)epoch) | ((u64)(unsigned)__builtin_amdgcn_readfirstlane((int)(unsigned)(epoch >> 32)) << 32)) + 1;
    const unsigned gsel = (w == 0) ? gwb : gwa;
    const unsigned vg1 = ((lane & 31) >> 2) * 9216 + (lane >> 5) * 64 + (lane & 3) * 16;

    f32x16 aQK = {}, aVL = {};
    float sxx = 0.f;
    const unsigned ax0 = ((32 * t + r) >> 3) * 1024 + (r & 7) * 128 + ((h ^ ((r >> 1) & 7)) << 4);
    unsigned aqk, avl;
    {
      int lo1 = (blk ? 96 : 0) + (u ? 40 : 0) + 8 * p4;
      int lo2 = blk ? (p4 == 0 ? 32 : (p4 == 1 ? 128 : (p4 == 2 ? 208 : 80))) : 176 + (u ? 40 : 0) + 8 * p4;
      aqk = 16384 + h * 2048 + q4 * 256 + ((lo1 + 64 * q4) & 255);
      avl = 16384 + h * 2048 + q4 * 256 + ((lo2 + 64 * q4) & 255);
    }
    unsigned axl = ax0, axh = ax0 + 65536, aqkl = aqk, aqkh = aqk + 65536, avll = avl, avlh = avl + 65536;
    unsigned axs[4][2];
#pragma unroll
    for (int s4 = 0; s4 < 4; ++s4) { axs[s4][0] = ax0 ^ (s4 << 5); axs[s4][1] = axs[s4][0] + 65536; asm volatile("" : "+v"(axs[s4][0]), "+v"(axs[s4][1])); }
    asm volatile("" : "+v"(aqkl), "+v"(aqkh), "+v"(avll), "+v"(avlh));
    (void)axl; (void)axh;
    u32x4 fa1[3], fa2[3];
    auto qkv_reads = [&](int j) {
      const int p = j >> 2, s4 = j & 3;
      const int hi = (p >> 1) & 1;
      const unsigned base = (p & 1) * L_QKV_STAGE;
      xf[j] = lds_rd128(axs[s4][hi] + base);
      const unsigned a1 = (hi ? aqkh : aqkl) + base + s4 * 4096, a2 = (hi ? avlh : avll) + base + s4 * 4096;
      fa1[j % 3] = tr_frag(a1, a1 + 1024);
      fa2[j % 3] = tr_frag(a2, a2 + 1024);
    };
    auto qkv_stage_open = [&](int p) {
      if (p == 0) { WB_QKV(12); STAMP(1); }
      else if (p <= 6) { WB_QKV(8); }
      else if (p == 7) { WB_QKV(4); }
      else { WB_QKV(3); }
      if (p >= 1 && p <= 5) { if (w == ((p + 3) & 7)) write_gx(p + 3, gsel); }
    };
    qkv_stage_open(0);
    qkv_reads(0); qkv_reads(1);
    SCHED_FENCE;
#pragma unroll
    for (int j = 0; j < 36; ++j) {
      if (j + 2 < 36) {
        if (((j + 2) & 3) == 0) { qkv_stage_open((j + 2) >> 2); SCHED_FENCE; }
        qkv_reads(j + 2);
      }
      {
        const int po = (j + 2) >> 2, k = (j + 2) & 3;
        if (po >= 1 && po <= 5) issue_qkvk(po + 3, k);
        if (po == 7 && k < 3) issue_g1k(0, 0, k, vg1);
        if (po == 8 && k < 3) issue_g1k(0, 1, k, vg1);
      }
      SCHED_FENCE;
      aQK = mfma(fa1[j % 3], xf[j], aQK);
      aVL = mfma(fa2[j % 3], xf[j], aVL);
#pragma unroll
      for (int e4 = 0; e4 < 4; ++e4) {
        const unsigned e = xf[j][e4];
        const bf16x2 eb = __builtin_bit_cast(bf16x2, e);
        sxx = __builtin_amdgcn_fdot2_f32_bf16(eb, eb, sxx, false);
      }
      SCHED_FENCE;
    }
    STAMP(2);
    qf0[0] = pack2(aQK[0], aQK[1]); qf0[1] = pack2(aQK[2], aQK[3]); qf0[2] = pack2(aQK[4], aQK[5]); qf0[3] = pack2(aQK[6], aQK[7]);
    u32x4 kf; kf[0] = pack2(aQK[8], aQK[9]); kf[1] = pack2(aQK[10], aQK[11]); kf[2] = pack2(aQK[12], aQK[13]); kf[3] = pack2(aQK[14], aQK[15]);
    const unsigned lf = u ? pack2(aVL[10], aVL[11]) : pack2(aVL[8], aVL[9]);
    const unsigned vl = u ? pack2(aVL[14], aVL[15]) : pack2(aVL[12], aVL[13]);
    qf1 = h ? 0u : lf;
    const float lg = hi_bcast(aVL[12]), sx = hi_bcast(aVL[13]);
    sxx = half_sum(sxx);
    gate = __builtin_amdgcn_rcpf(1.0f + __builtin_amdgcn_exp2f(-1.4426950409f * lg));
    omg = __builtin_amdgcn_rcpf(1.0f + __builtin_amdgcn_exp2f(1.4426950409f * lg));
    const float mean = sx * (1.0f / 576.0f);
    const float var = fmaxf(sxx * (1.0f / 576.0f) - mean * mean, 0.0f);
    const float rstd = __builtin_amdgcn_rsqf(var + 1e-5f);
    const float nb = -mean * rstd;
    const unsigned vs = L_VSCR + w * 1280;
    u32x4 vm; vm[0] = pack2(aVL[0], aVL[1]); vm[1] = pack2(aVL[2], aVL[3]); vm[2] = pack2(aVL[4], aVL[5]); vm[3] = pack2(aVL[6], aVL[7]);
    lds_wr128(vs + r * 32 + h * 16, vm);
    if (h == 0) { u32x2 v2; v2[0] = vl; v2[1] = 0x00003F80u; lds_wr64(vs + 1024 + r * 8, v2); }
    asm volatile("s_waitcnt lgkmcnt(0)" ::: "memory");
    const int kb = 4 * m + t;
    u32x4 vt[2];
#pragma unroll
    for (int sp = 0; sp < 2; ++sp) {
      int key0 = 16 * sp + 4 * h + q4, key1 = key0 + 8;
      unsigned a0 = blk ? vs + 1024 + key0 * 8 : vs + key0 * 32 + p4 * 8;
      unsigned a1 = blk ? vs + 1024 + key1 * 8 : vs + key1 * 32 + p4 * 8;
      vt[sp] = tr_frag(a0, a1);
    }
    {
      const size_t hb = (size_t)head * 64 + kb;
      const rsrc_t rk = mk_rsrc(P.ws + WS_K16 + hb * 1024, 1024), rkl = mk_rsrc(P.ws + WS_KL + hb * 128, 128), rv = mk_rsrc(P.ws + WS_VT + hb * 2048, 2048);
      __builtin_amdgcn_raw_buffer_store_b128(kf, rk, lane * 16, 0, 16);
      __builtin_amdgcn_raw_buffer_store_b32(lf, rkl, h ? r * 4 : 0x40000000, 0, 16);
      __builtin_amdgcn_raw_buffer_store_b128(vt[0], rv, lane * 16, 0, 16);
      __builtin_amdgcn_raw_buffer_store_b128(vt[1], rv, lane * 16 + 1024, 0, 16);
    }
    ln_rstd = rstd; ln_nb = nb;
  }
  STAMP(3);

  GAS u64* myflag = (GAS u64*)(P.ws + WS_KVFLAG + (size_t)(m * 16 + s) * 128);
  {
    LANEVARS
    const unsigned vg1 = ((lane & 31) >> 2) * 9216 + (lane >> 5) * 64 + (lane & 3) * 16;
    const unsigned vg8 = (lane >> 2) * 9216 + (lane & 3) * 16;
    auto issue_g8k = [&](int hc, int k) {
      unsigned base = L_G1 + hc * 24576;
      if (k < 2) {
        int g = 2 * w + k;
        dma16(rs_w1, vg8, (288 * hc + 16 * g) * 9216 + (288 * s + 256) * 2, base + g * 1024);
      } else {
        int g = 16 + (w >> 2);
        unsigned so = (288 * hc + 16 * g) * 9216 + (288 * s + 256) * 2, lb = base + g * 1024;
        asm volatile("" : "+s"(so), "+s"(lb));
        if ((lane >> 4) == (w & 3)) dma16(rs_w1, vg8, so, lb);
      }
    };
    unsigned ag1a = L_G1 + h * 1024 + u * 512 + q4 * 64 + blk * 32 + p4 * 8;
    unsigned ag1b = ag1a + 49152;
    asm volatile("" : "+v"(ag1a), "+v"(ag1b));
    auto normalize = [&](int j) {
#pragma unroll
      for (int e4 = 0; e4 < 4; ++e4) {
        const unsigned v = xf[j][e4];
        unsigned y = pack2(__builtin_fmaf(bf_lo(v), ln_rstd, ln_nb), __builtin_fmaf(bf_hi(v), ln_rstd, ln_nb));
        asm volatile("" : "+v"(y));
        xf[j][e4] = y;
      }
    };
    unsigned ag8 = L_G1 + h * 512 + q4 * 64 + blk * 32 + p4 * 8;
    unsigned ahs = L_H + t * 2048 + lane * 16, ab1 = L_B1 + h * 16;
    asm volatile("" : "+v"(ag8), "+v"(ahs), "+v"(ab1));
    auto bias_init = [&](int fb) {
      f32x16 a;
#pragma unroll
      for (int g = 0; g < 4; ++g) {
        f32x4 v = lds_rdf4(ab1 + (32 * fb + 8 * g) * 4);
        a[4 * g] = v[0]; a[4 * g + 1] = v[1]; a[4 * g + 2] = v[2]; a[4 * g + 3] = v[3];
      }
      return a;
    };
    auto gelu_store = [&](const f32x16& a, int fb) {
      u32x4 f0, f1;
#pragma unroll
      for (int j = 0; j < 4; ++j) {
        f0[j] = pack2(gelu_scaled(a[2 * j], omg), gelu_scaled(a[2 * j + 1], omg));
        f1[j] = pack2(gelu_scaled(a[8 + 2 * j], omg), gelu_scaled(a[8 + 2 * j + 1], omg));
      }
      lds_wr128(ahs + fb * 8192, f0); lds_wr128(ahs + fb * 8192 + 1024, f1);
    };
    u32x4 fr[4];
    f32x16 acc, accp;
    float gt[16], gx[16];
    u32x4 hq0, hq1;
    auto g1_read = [&](int j) {
      const unsigned a = (j < 24 ? ag1a + (j / 12) * 24576 : ag1b) + (j % 12) * 2048;
      fr[j & 3] = tr_frag(a, a + 256);
    };
    auto gelu_slot = [&](int sl) {
      if (sl < 32) {
        const int e = sl >> 1;
        if ((sl & 1) == 0) {
          const float x = accp[e];
          const float x2 = x * x;
          gx[e] = x * omg;
          gt[e] = __builtin_amdgcn_exp2f(x * __builtin_fmaf(x2, -0.1029432397f, -2.3022081986f));
          asm volatile("" : "+v"(gx[e]), "+v"(gt[e]));
        } else {
          gx[e] = gx[e] * __builtin_amdgcn_rcpf(1.0f + gt[e]);
          if (e & 1) {
            const unsigned pk = pack2(gx[e - 1], gx[e]);
            unsigned pkk = pk; asm volatile("" : "+v"(pkk));
            if (e < 8) hq0[e >> 1] = pkk; else hq1[(e - 8) >> 1] = pkk;
          } else { asm volatile("" : "+v"(gx[e])); }
        }
      }
    };
    auto h_store = [&](int fb) {
      lds_wr128(ahs + fb * 8192, hq0); lds_wr128(ahs + fb * 8192 + 1024, hq1);
    };
    WB_G1(7);
    issue_g1k(0, 2, 0, vg1);
    acc = bias_init(u);
    g1_read(0); g1_read(1); g1_read(2);
    normalize(0);
    SCHED_FENCE;
#pragma unroll
    for (int sl = 0; sl < 36; ++sl) {
      if (sl == 9) { WB_G1(7); SCHED_FENCE; }
      if (sl == 21) { WB_G1(3);
        if (w == 0 && lane == 0) __hip_atomic_store(myflag, ep1, __ATOMIC_RELAXED, __HIP_MEMORY_SCOPE_AGENT);
        SCHED_FENCE; }
      if (sl == 33) { WB_G1(3); SCHED_FENCE; }
      if (sl == 1 || sl == 5) issue_g1k(0, 2, (sl + 3) / 4, vg1);
      if (sl == 9 || sl == 13 || sl == 17) issue_g1k(1, 0, (sl - 9) / 4, vg1);
      if (sl == 21 || sl == 25 || sl == 29) issue_g1k(1, 1, (sl - 21) / 4, vg1);
      if (sl == 33) issue_g1k(1, 2, 0, vg1);
      g1_read((sl + 3) % 36);
      if (sl + 1 < 36) normalize(sl + 1);
      SCHED_FENCE;
      acc = mfma(fr[sl & 3], xf[sl], acc);
      SCHED_FENCE;
    }
    STAMP(10);
#pragma unroll
    for (int i = 1; i < 4; ++i) {
      accp = acc;
      acc = bias_init(2 * i + u);
      SCHED_FENCE;
#pragma unroll
      for (int sl = 0; sl < 36; ++sl) {
        if (sl == 9 || sl == 21) { WB_G1(3); SCHED_FENCE; }
        if (sl == 33) { if (i < 3) { WB_G1(3); } else { WB_G1(0); } SCHED_FENCE; }
        if (sl == 1 || sl == 5) issue_g1k(i, 2, (sl + 3) / 4, vg1);
        if (sl == 9 || sl == 13 || sl == 17) { if (i < 3) issue_g1k(i + 1, 0, (sl - 9) / 4, vg1); else issue_g8k(0, (sl - 9) / 4); }
        if (sl == 21 || sl == 25 || sl == 29) { if (i < 3) issue_g1k(i + 1, 1, (sl - 21) / 4, vg1); else issue_g8k(1, (sl - 21) / 4); }
        if (sl == 33 && i < 3) issue_g1k(i + 1, 2, 0, vg1);
        if (sl < 33) g1_read(sl + 3);
        else {
          const unsigned a = (i < 3) ? (ag1a + (sl - 33) * 2048) : (ag8 + (sl - 33) * 1024);
          fr[(sl + 3) & 3] = tr_frag(a, a + 256);
        }
        gelu_slot(sl);
        if (sl == 33) h_store(2 * (i - 1) + u);
        SCHED_FENCE;
        acc = mfma(fr[sl & 3], xf[sl], acc);
        SCHED_FENCE;
      }
    }
    STAMP(4);
    accp = acc;
    if (u == 0) {
      acc = bias_init(8);
      SCHED_FENCE;
#pragma unroll
      for (int sl = 0; sl < 36; ++sl) {
        if (sl + 3 < 36) {
          const int j = sl + 3;
          const unsigned a = ag8 + (j / 18) * 24576 + (j % 18) * 1024;
          fr[j & 3] = tr_frag(a, a + 256);
        }
        gelu_slot(sl);
        if (sl == 33) h_store(6);
        SCHED_FENCE;
        acc = mfma(fr[sl & 3], xf[sl], acc);
        SCHED_FENCE;
      }
      gelu_store(acc, 8);
    } else {
      gelu_store(accp, 7);
    }
  }
  const i32x4 rs_w2 = make_srd(P.w2, FF * DM * 2);
  const i32x4 rs_wo = make_srd(P.wo, DM * DM * 2);
  const i32x4 rs_b2 = make_srd(P.b2, DM * 2);
  auto issue_g2k = [&](int i, unsigned base, int k, int lane) {
    const int row8 = (lane & 31) >> 2;
    const unsigned cc = (lane >> 5) * 64 + (lane & 3) * 16;
    const unsigned vg2 = row8 * 1152 + cc;
    if (k < 4) {
      int g = w + 8 * k;
      dma16(rs_w2, vg2, (288 * s + 8 * g) * 1152 + i * 128, base + g * 1024);
    } else if (k == 4) {
      if (w < 4) dma16(rs_w2, vg2, (288 * s + 8 * (32 + w)) * 1152 + i * 128, base + (32 + w) * 1024);
      else {
        const unsigned vo = (8 * (row8 >> 2) + (row8 & 3) + ((w & 1) ? 4 : 0)) * 1152 + cc;
        dma16(rs_wo, vo, (36 * s + ((w & 2) ? 20 : 0)) * 1152 + i * 128, base + (32 + w) * 1024);
      }
    } else {
      unsigned so = ((w & 2) ? 0 : (36 * s) * 1152) + i * 128, lb = base + 40 * 1024;
      asm volatile("" : "+s"(so), "+s"(lb));
      const unsigned vl2 = ((w & 2) ? 0 : (16 + (row8 & 3)) * 1152) + cc;
      i32x4 srd = (w & 2) ? rs_b2 : rs_wo;
      asm volatile("" : "+s"(srd));
      if ((lane >> 3) == w) dma16(srd, vl2, so, lb);
    }
  };
  auto issue_g2 = [&](int i, unsigned base, int lane) {
#pragma unroll
    for (int k = 0; k < 6; ++k) issue_g2k(i, base, k, lane);
  };
  WAIT_BAR(0);
  STAMP(5);

  {
    LANEVARS
    issue_g2(0, L_G2A, lane);
    const int q0b = 4 * m + t;
    {
      GAS u64* f0 = (GAS u64*)(P.ws + WS_KVFLAG + (size_t)((m >= 2 ? m - 2 : m) * 16 + s) * 128);
      GAS u64* f1 = (GAS u64*)(P.ws + WS_KVFLAG + (size_t)((m >= 1 ? m - 1 : m) * 16 + s) * 128);
      unsigned spins = 0;
      while (true) {
        u64 a = __hip_atomic_load(f0, __ATOMIC_RELAXED, __HIP_MEMORY_SCOPE_AGENT);
        u64 bb = __hip_atomic_load(f1, __ATOMIC_RELAXED, __HIP_MEMORY_SCOPE_AGENT);
        u64 c = __hip_atomic_load(myflag, __ATOMIC_RELAXED, __HIP_MEMORY_SCOPE_AGENT);
        bool ok = (a == ep1) && (bb == ep1) && (c == ep1);
        if (__builtin_amdgcn_readfirstlane((int)ok)) break;
        if (++spins > (1u << 20)) break;
        __builtin_amdgcn_s_sleep(2);
      }
    }
    asm volatile("" ::: "memory");
    STAMP(6);
    const rsrc_t rk = mk_rsrc(P.ws + WS_K16, 32 * 64 * 1024), rkl = mk_rsrc(P.ws + WS_KL, 32 * 64 * 128), rv = mk_rsrc(P.ws + WS_VT, 32 * 64 * 2048);
    f32x16 S[9];
    const int nskip = q0b >= 8 ? 0 : 8 - q0b;
    u32x4 qf1v; qf1v[0] = qf1; qf1v[1] = 0; qf1v[2] = 0; qf1v[3] = 0;
    {
      u32x4 kf[9]; unsigned kl[9];
#pragma unroll
      for (int kt = 0; kt < 9; ++kt) {
        int kb = q0b - 8 + kt; kb = kb < 0 ? 0 : kb;
        kf[kt] = __builtin_amdgcn_raw_buffer_load_b128(rk, lane * 16, (head * 64 + kb) * 1024, 16);
        kl[kt] = __builtin_amdgcn_raw_buffer_load_b32(rkl, r * 4, (head * 64 + kb) * 128, 16);
      }
#pragma unroll
      for (int kt = 0; kt < 9; ++kt) {
        f32x16 z = {};
        u32x4 klv; klv[0] = h ? 0u : kl[kt]; klv[1] = 0; klv[2] = 0; klv[3] = 0;
        z = mfma(kf[kt], qf0, z);
        S[kt] = mfma(klv, qf1v, z);
      }
    }
    SCHED_FENCE;
    u32x4 vf[9][2];
    const unsigned vvo = (r < 19) ? lane * 16 : 0x40000000;
#pragma unroll
    for (int kt = 0; kt < 9; ++kt) {
      int kb = q0b - 8 + kt; kb = kb < 0 ? 0 : kb;
      vf[kt][0] = __builtin_amdgcn_raw_buffer_load_b128(rv, vvo, (head * 64 + kb) * 2048, 16);
      vf[kt][1] = __builtin_amdgcn_raw_buffer_load_b128(rv, vvo, (head * 64 + kb) * 2048 + 1024, 16);
    }
    SCHED_FENCE;
    const float NEG = -1e30f;
#pragma unroll
    for (int i = 0; i < 16; ++i) {
      int row = (i & 3) + 8 * (i >> 2) + 4 * h;
      S[0][i] = (row > r) ? S[0][i] : NEG;
      S[8][i] = (row <= r) ? S[8][i] : NEG;
    }
    if (nskip > 0) {
#pragma unroll
      for (int kt = 0; kt < 8; ++kt) {
        if (kt < nskip) {
#pragma unroll
          for (int i = 0; i < 16; ++i) S[kt][i] = NEG;
        }
      }
    }
    float mx = NEG;
#pragma unroll
    for (int kt = 0; kt < 9; ++kt)
#pragma unroll
      for (int i = 0; i < 16; ++i) mx = fmaxf(mx, S[kt][i]);
    mx = half_max(mx);
    const float csc = 0.23570226039551584f * 1.4426950408889634f;
    const float mc = -mx * csc;
    f32x16 O = {};
#pragma unroll
    for (int kt = 0; kt < 9; ++kt) {
      u32x4 p0, p1;
#pragma unroll
      for (int j = 0; j < 4; ++j) {
        p0[j] = pack2(__builtin_amdgcn_exp2f(__builtin_fmaf(S[kt][2 * j], csc, mc)), __builtin_amdgcn_exp2f(__builtin_fmaf(S[kt][2 * j + 1], csc, mc)));
        p1[j] = pack2(__builtin_amdgcn_exp2f(__builtin_fmaf(S[kt][8 + 2 * j], csc, mc)), __builtin_amdgcn_exp2f(__builtin_fmaf(S[kt][8 + 2 * j + 1], csc, mc)));
      }
      O = mfma(vf[kt][0], p0, O);
      O = mfma(vf[kt][1], p1, O);
    }
    const float sc = gate * __builtin_amdgcn_rcpf(lo_bcast(O[10]));
    u32x4 of; of[0] = pack2(O[0] * sc, O[1] * sc); of[1] = pack2(O[2] * sc, O[3] * sc); of[2] = pack2(O[4] * sc, O[5] * sc); of[3] = pack2(O[6] * sc, O[7] * sc);
    lds_wr128(L_OX + (t * 2 + u) * 1024 + lane * 16, of);
    if (h == 0) lds_wr32(L_OL + (t * 2 + u) * 128 + r * 4, pack2(O[8] * sc, O[9] * sc));
  }
  STAMP(7);
  WAIT_BAR(0);
  STAMP(8);
  {
    LANEVARS
    u32x4 hf[21];
#pragma unroll
    for (int fb = 0; fb < 9; ++fb) {
      hf[2 * fb] = lds_rd128(L_H + (fb * 4 + t) * 2048 + lane * 16);
      hf[2 * fb + 1] = lds_rd128(L_H + (fb * 4 + t) * 2048 + 1024 + lane * 16);
    }
    hf[18] = lds_rd128(L_OX + (t * 2 + 0) * 1024 + lane * 16);
    hf[19] = lds_rd128(L_OX + (t * 2 + 1) * 1024 + lane * 16);
    {
      unsigned la = lds_rd32(L_OL + (t * 2 + 0) * 128 + r * 4), lb = lds_rd32(L_OL + (t * 2 + 1) * 128 + r * 4);
      const unsigned oh = pack2(omg, 0.f) & 0xffffu;
      const unsigned ol = pack2(omg - bf_lo(oh), 0.f) & 0xffffu;
      const unsigned ob = (s == 0) ? (oh | (ol << 16)) : 0u;
      hf[20][0] = h ? ob : la; hf[20][1] = h ? 0u : lb; hf[20][2] = 0; hf[20][3] = 0;
    }
    WAIT_BAR(0);
    issue_g2(1, L_G2B, lane); issue_g2(2, L_G2C, lane);
    unsigned ag2 = u * 512 + (4 * h + q4) * 64 + blk * 32 + p4 * 8;
    unsigned ag2A = ag2 + L_G2A, ag2B = ag2 + L_G2B, ag2C = ag2 + L_G2C;
    asm volatile("" : "+v"(ag2A), "+v"(ag2B), "+v"(ag2C));
    char* const partbase = P.ws + WS_PART + (size_t)(m * 16 + s) * 18 * 4 * 2048;
    u32x4 fr[4];
    f32x16 acc, accp;
    auto g2_read = [&](unsigned slotbase, int j) {
      const unsigned a = slotbase + (j < 20 ? j * 2048 : 40 * 1024);
      fr[j & 3] = tr_frag(a, j < 20 ? a + 1024 : a);
    };
    auto g2_store = [&](const f32x16& a, int db, int half) {
      u32x4 o;
#pragma unroll
      for (int j = 0; j < 4; ++j) o[j] = pack2(a[8 * half + 2 * j], a[8 * half + 2 * j + 1]);
      const rsrc_t rq = mk_rsrc(partbase + (size_t)(db * 4 + t) * 2048, 2048);
      __builtin_amdgcn_raw_buffer_store_b128(o, rq, lane * 16 + half * 1024, 0, 0);
    };
    g2_read(ag2A, 0); g2_read(ag2A, 1); g2_read(ag2A, 2);
    SCHED_FENCE;
#pragma unroll
    for (int i = 0; i < 9; ++i) {
      const unsigned cur = (i % 3 == 0) ? L_G2A : ((i % 3 == 1) ? L_G2B : L_G2C);
      const unsigned prv = ((i + 2) % 3 == 0) ? L_G2A : (((i + 2) % 3 == 1) ? L_G2B : L_G2C);
      const unsigned cura = (i % 3 == 0) ? ag2A : ((i % 3 == 1) ? ag2B : ag2C);
      const unsigned nxta = ((i + 1) % 3 == 0) ? ag2A : (((i + 1) % 3 == 1) ? ag2B : ag2C);
      const int rot = (i * 21) & 3;
#pragma unroll
      for (int sl = 0; sl < 21; ++sl) {
        if (sl == 18 && i < 8) {
          if (i == 0) { WB_G2(6); } else if (i <= 6) { WB_G2(8); } else { WB_G2(2); }
          SCHED_FENCE;
        }
        if (sl == 18 && i + 3 < 9) issue_g2k(i + 3, cur, 0, lane);
        if (i >= 1 && i + 2 < 9 && (sl == 0 || sl == 3 || sl == 6 || sl == 9 || sl == 12)) issue_g2k(i + 2, prv, 1 + sl / 3, lane);
        if (sl + 3 < 21) {
          const int j = sl + 3;
          const unsigned a = cura + (j < 20 ? j * 2048 : 40 * 1024);
          fr[(rot + j) & 3] = tr_frag(a, j < 20 ? a + 1024 : a);
        } else if (i < 8) {
          const int j = sl + 3 - 21;
          const unsigned a = nxta + j * 2048;
          fr[(rot + 21 + j) & 3] = tr_frag(a, a + 1024);
        }
        if (i > 0 && sl == 1) g2_store(accp, 2 * (i - 1) + u, 0);
        if (i > 0 && sl == 3) g2_store(accp, 2 * (i - 1) + u, 1);
        SCHED_FENCE;
        if (sl == 0) { if (i > 0) accp = acc; f32x16 z = {}; acc = mfma(fr[(rot + sl) & 3], hf[sl], z); }
        else acc = mfma(fr[(rot + sl) & 3], hf[sl], acc);
        SCHED_FENCE;
      }
    }
    g2_store(acc, 16 + u, 0); g2_store(acc, 16 + u, 1);
  }
  STAMP(9);
  if (b == 0 && w == 0) {
    const int lane = fresh_lane();
    unsigned spins = 0;
    while (true) {
      bool ok = true;
#pragma unroll
      for (int k = 0; k < 4; ++k) {
        u64 v = __hip_atomic_load((GAS u64*)(P.ws + WS_KVFLAG + (size_t)(lane * 4 + k) * 128), __ATOMIC_RELAXED, __HIP_MEMORY_SCOPE_AGENT);
        ok &= (v == ep1);
      }
      if (__all(ok)) break;
      if (++spins > (1u << 20)) break;
      __builtin_amdgcn_s_sleep(2);
    }
    if (lane == 0) __hip_atomic_store((GAS u64*)(P.ws + WS_EPOCH), ep1, __ATOMIC_RELAXED, __HIP_MEMORY_SCOPE_AGENT);
  }
}

__global__ __launch_bounds__(576) void reduce_kernel(const char* __restrict__ part, unsigned short* __restrict__ out) {
  const int tid = threadIdx.x, b = blockIdx.x;
  const int m = b >> 4, j = b & 15;
  const int db = tid >> 5, qq = (tid >> 4) & 1, h = (tid >> 3) & 1, e = tid & 7;
  const int t = j >> 2, r = 8 * (j & 3) + e;
  const char* p = part + (((size_t)(m * 16) * 18 + db) * 4 + t) * 2048 + qq * 1024 + (h * 32 + r) * 16;
  float a[8] = {};
  u32x4 v[16];
#pragma unroll
  for (int s = 0; s < 16; ++s) v[s] = *(const u32x4*)(p + (size_t)s * 18 * 4 * 2048);
#pragma unroll
  for (int s = 0; s < 16; ++s)
#pragma unroll
    for (int k = 0; k < 4; ++k) { const unsigned e2 = v[s][k]; a[2 * k] += bf_lo(e2); a[2 * k + 1] += bf_hi(e2); }
  unsigned short* o = out + (size_t)(128 * m + 32 * t + r) * DM + 32 * db + 16 * qq + 4 * h;
  u32x2 o0, o1; o0[0] = pack2(a[0], a[1]); o0[1] = pack2(a[2], a[3]); o1[0] = pack2(a[4], a[5]); o1[1] = pack2(a[6], a[7]);
  *(u32x2*)o = o0; *(u32x2*)(o + 8) = o1;
}

extern "C" void kernel_launch(void* const* d_in, const int* in_sizes, int n_in, void* d_out, int out_size, void* d_ws, size_t ws_size, hipStream_t stream) {
  Params P;
  P.x = (const unsigned short*)d_in[0]; P.wqkv = (const unsigned short*)d_in[1]; P.wo = (const unsigned short*)d_in[2];
  P.w1 = (const unsigned short*)d_in[4]; P.b1 = (const unsigned short*)d_in[5]; P.w2 = (const unsigned short*)d_in[6];
  P.b2 = (const unsigned short*)d_in[7]; P.gw = (const unsigned short*)d_in[8];
  P.ws = (char*)d_ws; P.out = (unsigned short*)d_out;
  fused_kernel<<<256, 512, 0, stream>>>(P);
  reduce_kernel<<<256, 576, 0, stream>>>((const char*)d_ws + WS_PART, (unsigned short*)d_out);
}
```

```cpp
#include <hip/hip_runtime.h>
#include <stdint.h>

typedef __attribute__((ext_vector_type(8))) __bf16 bf16x8;
typedef __attribute__((ext_vector_type(2))) __bf16 bf16x2;
typedef __attribute__((ext_vector_type(4))) short s16x4;
typedef __attribute__((ext_vector_type(16))) float f32x16;
typedef __attribute__((ext_vector_type(4))) float f32x4;
typedef __attribute__((ext_vector_type(4))) unsigned u32x4;
typedef __attribute__((ext_vector_type(2))) unsigned u32x2;
typedef __attribute__((ext_vector_type(4))) int i32x4;
typedef __amdgpu_buffer_rsrc_t rsrc_t;
typedef unsigned long long u64;
#define LAS __attribute__((address_space(3)))
#define GAS __attribute__((address_space(1)))
#define DI __device__ __forceinline__

constexpr int SEQ = 2048, DM = 576, FF = 4608;
constexpr size_t WS_EPOCH = 0;
constexpr size_t WS_KVFLAG = 4096;
constexpr size_t WS_S1FLAG = WS_KVFLAG + 32768;
constexpr size_t WS_PFLAG = WS_S1FLAG + 32768;
constexpr size_t WS_K16 = 131072;
constexpr size_t WS_KL = WS_K16 + 32 * 64 * 1024;
constexpr size_t WS_VT = WS_KL + 32 * 64 * 128;
constexpr size_t WS_S1P = WS_VT + 32 * 64 * 2048;
constexpr size_t WS_PART = WS_S1P + 16 * 16 * 2304;
constexpr size_t WS_END = WS_PART + (size_t)16 * 16 * 18 * 4 * 2 * 1024;

constexpr unsigned L_QKV_STAGE = 32768;
constexpr unsigned L_S1SCR = 131072;
constexpr unsigned L_VSCR = 0;
constexpr unsigned L_GATH = 32768;
constexpr unsigned L_H = 0;
constexpr unsigned L_G1 = 73728;
constexpr unsigned L_G2A = 73728, L_G2B = 0, L_G2C = 115712;
constexpr unsigned L_OX = 131072;
constexpr unsigned L_OL = L_OX + 8192;
constexpr unsigned L_T = 157696;
constexpr unsigned L_SXX = L_T + 4608;
constexpr unsigned L_DUMMY = L_SXX + 512;
constexpr unsigned L_GO = L_DUMMY + 256;
constexpr unsigned L_END = L_GO + 16;
static_assert(L_END <= 163840, "lds");

DI unsigned pack2(float a, float b) { bf16x2 v; v[0] = (__bf16)a; v[1] = (__bf16)b; return __builtin_bit_cast(unsigned, v); }
DI float u2f(unsigned u) { return __builtin_bit_cast(float, u); }
DI unsigned f2u(float f) { return __builtin_bit_cast(unsigned, f); }
DI float bf_lo(unsigned u) { return u2f(u << 16); }
DI float bf_hi(unsigned u) { return u2f(u & 0xffff0000u); }
DI f32x16 mfma(u32x4 a, u32x4 b, f32x16 c) {
  return __builtin_amdgcn_mfma_f32_32x32x16_bf16(__builtin_bit_cast(bf16x8, a), __builtin_bit_cast(bf16x8, b), c, 0, 0, 0);
}
DI i32x4 make_srd(const void* p, unsigned bytes) {
  u64 a = (u64)p; i32x4 r;
  r[0] = __builtin_amdgcn_readfirstlane((int)(unsigned)a);
  r[1] = __builtin_amdgcn_readfirstlane((int)((unsigned)(a >> 32) & 0xffffu));
  r[2] = (int)bytes; r[3] = 0x00020000;
  return r;
}
DI rsrc_t mk_rsrc(const void* p, unsigned bytes) { return __builtin_amdgcn_make_buffer_rsrc((void*)p, 0, bytes, 0x00020000); }
DI void dma16(i32x4 srd, unsigned voff, unsigned soff, unsigned lds) {
  asm volatile("s_mov_b32 m0, %0\n\ts_nop 0\n\tbuffer_load_dwordx4 %1, %2, %3 offen lds" ::"s"(lds), "v"(voff), "s"(srd), "s"(soff));
}
template <unsigned LC, unsigned SC>
DI void dma16i(i32x4 srd, unsigned voff, unsigned sb, unsigned lb) {
  unsigned tmp;
  asm volatile("s_add_u32 m0, %1, %5\n\ts_add_u32 %0, %2, %6\n\tbuffer_load_dwordx4 %3, %4, %0 offen lds"
               : "=&s"(tmp) : "s"(lb), "s"(sb), "v"(voff), "s"(srd), "i"(LC), "i"(SC) : "scc");
}
template <unsigned C>
DI void dma16i_sc1(i32x4 srd, unsigned voff, unsigned sb, unsigned lb) {
  unsigned tmp;
  asm volatile("s_add_u32 m0, %5, %4\n\ts_add_u32 %0, %1, %4\n\tbuffer_load_dwordx4 %2, %3, %0 offen sc1 lds" : "=&s"(tmp) : "s"(sb), "v"(voff), "s"(srd), "i"(C), "s"(lb) : "scc");
}
DI void touch4(i32x4 srd, unsigned voff, unsigned soff) {
  asm volatile("s_mov_b32 m0, %0\n\ts_nop 0\n\tbuffer_load_dword %1, %2, %3 offen lds" ::"s"(L_DUMMY), "v"(voff), "s"(srd), "s"(soff));
}
#define WAIT_BAR(N) asm volatile("s_waitcnt vmcnt(" #N ") lgkmcnt(0)\n\ts_barrier" ::: "memory")
#define WAIT_VM(N) asm volatile("s_waitcnt vmcnt(" #N ")" ::: "memory")
#define BAR_LGKM() asm volatile("s_waitcnt lgkmcnt(0)\n\ts_barrier" ::: "memory")
#define WAIT_BAR_U0(N) do { if (u == 0) WAIT_VM(N); BAR_LGKM(); } while (0)
#define SCHED_FENCE __builtin_amdgcn_sched_barrier(0)
#define MFMA_SETTLE() asm volatile("s_nop 7\n\ts_nop 5")
#define SPIN_S(x) asm volatile("" : "+s"(x))
#define PIN(x) asm volatile("" : "+v"(x))

__shared__ __attribute__((aligned(16))) char smem[163840];
DI u32x4 lds_rd128(unsigned a) { return *(LAS u32x4*)((LAS char*)smem + a); }
DI void lds_wr128(unsigned a, u32x4 v) { *(LAS u32x4*)((LAS char*)smem + a) = v; }
DI void lds_wr64(unsigned a, u32x2 v) { *(LAS u32x2*)((LAS char*)smem + a) = v; }
DI void lds_wr32(unsigned a, unsigned v) { *(LAS unsigned*)((LAS char*)smem + a) = v; }
DI void lds_wrf4(unsigned a, f32x4 v) { *(LAS f32x4*)((LAS char*)smem + a) = v; }
DI void lds_wr16(unsigned a, unsigned v) { *(LAS unsigned short*)((LAS char*)smem + a) = (unsigned short)v; }
DI void lds_add32(unsigned a, unsigned v) { asm volatile("ds_add_u32 %0, %1" :: "v"(a), "v"(v) : "memory"); }
DI unsigned lds_ld32v(unsigned a) { return *(volatile LAS unsigned*)((LAS char*)smem + a); }
DI unsigned lds_rd32(unsigned a) { return *(LAS unsigned*)((LAS char*)smem + a); }
DI float lds_rdf(unsigned a) { return *(LAS float*)((LAS char*)smem + a); }
DI f32x4 lds_rdf4(unsigned a) { return *(LAS f32x4*)((LAS char*)smem + a); }
DI u32x4 tr_frag(unsigned a0, unsigned a1) {
  s16x4 lo = __builtin_amdgcn_ds_read_tr16_b64_v4i16((LAS s16x4*)((LAS char*)smem + a0));
  s16x4 hi = __builtin_amdgcn_ds_read_tr16_b64_v4i16((LAS s16x4*)((LAS char*)smem + a1));
  return __builtin_bit_cast(u32x4, __builtin_shufflevector(lo, hi, 0, 1, 2, 3, 4, 5, 6, 7));
}
DI int fresh_lane() { int l; asm volatile("v_mbcnt_lo_u32_b32 %0, -1, 0\n\tv_mbcnt_hi_u32_b32 %0, -1, %0" : "=v"(l)); return l; }
#define LANEVARS const int lane = fresh_lane(); const int r = lane & 31, h = lane >> 5; const int blk = (lane >> 4) & 1, q4 = (lane & 15) >> 2, p4 = lane & 3; (void)r; (void)h; (void)blk; (void)q4; (void)p4;
DI float lo_bcast(float v) { unsigned x = f2u(v); auto p = __builtin_amdgcn_permlane32_swap(x, x, false, false); unsigned y = p[0]; return u2f(y); }
DI float hi_bcast(float v) { unsigned x = f2u(v); auto p = __builtin_amdgcn_permlane32_swap(x, x, false, false); unsigned y = p[1]; return u2f(y); }
DI float half_sum(float v) { unsigned x = f2u(v); auto p = __builtin_amdgcn_permlane32_swap(x, x, false, false); unsigned y0 = p[0], y1 = p[1]; return u2f(y0) + u2f(y1); }
DI float half_max(float v) { unsigned x = f2u(v); auto p = __builtin_amdgcn_permlane32_swap(x, x, false, false); unsigned y0 = p[0], y1 = p[1]; return fmaxf(u2f(y0), u2f(y1)); }
DI u64 ld_flag(const char* p) { return __hip_atomic_load((GAS u64*)p, __ATOMIC_RELAXED, __HIP_MEMORY_SCOPE_AGENT); }
DI void st_flag(char* p, u64 v) { __hip_atomic_store((GAS u64*)p, v, __ATOMIC_RELAXED, __HIP_MEMORY_SCOPE_AGENT); }
constexpr unsigned OOB = 0x40000000u;
constexpr unsigned SPIN_CAP = 1u << 22;

struct Params {
  const unsigned short* x; const unsigned short* wqkv; const unsigned short* wo;
  const unsigned short* w1; const unsigned short* b1; const unsigned short* w2; const unsigned short* b2; const unsigned short* gw;
  char* ws; unsigned short* out;
};


DI float gelu_f(float x) {
  float x2 = x * x;
  float t = x * __builtin_fmaf(x2, -0.1029432397f, -2.3022081986f);
  float e = __builtin_amdgcn_exp2f(t);
  return x * __builtin_amdgcn_rcpf(1.0f + e);
}
template <int I> struct IC { static constexpr int value = I; };
template <int I, int N, class F> DI void static_for(F&& f) { if constexpr (I < N) { f(IC<I>{}); static_for<I + 1, N>(f); } }

struct Carry { u32x4 qf0; unsigned qf1; float gate, omg, omr; };


struct G2Dma {
  i32x4 rs_w2, rs_wo, rs_x;
  unsigned v_w2, v_wo, v_x;
  unsigned sb_w2, sb_wo, sb_x, lb;
  bool xact;
};
template <int CH, unsigned BASE, int K>
DI void issue_g2(const G2Dma& D) {
  if constexpr (K < 9) dma16i<BASE + 4096 * K, 32 * K * 1152 + CH * 128>(D.rs_w2, D.v_w2, D.sb_w2, D.lb);
  else if constexpr (K == 9) dma16i<BASE + 36 * 1024, CH * 128>(D.rs_wo, D.v_wo, D.sb_wo, D.lb);
  else { if (D.xact) dma16i<BASE + 40 * 1024, CH * 128>(D.rs_x, D.v_x, D.sb_x, 0u); }
}
DI G2Dma make_g2dma(const Params& P, int t, int s, int lane) {
  G2Dma D;
  D.rs_w2 = make_srd(P.w2, FF * DM * 2); D.rs_wo = make_srd(P.wo, DM * DM * 2);
  const i32x4 rs_b2 = make_srd(P.b2, DM * 2);
  D.rs_x = (t & 1) ? rs_b2 : D.rs_wo;
  const int row8 = (lane & 31) >> 2;
  const unsigned cc = (lane >> 5) * 64 + (lane & 3) * 16;
  D.v_w2 = row8 * 1152 + cc;
  D.v_wo = (8 * (row8 >> 2) + (row8 & 3) + ((t & 1) ? 4 : 0)) * 1152 + cc;
  D.v_x = ((t & 1) ? 0 : (16 + (row8 & 3)) * 1152) + cc;
  D.sb_w2 = (288 * s + 8 * t) * 1152; D.sb_wo = (36 * s + ((t & 2) ? 20 : 0)) * 1152; D.sb_x = (t & 1) ? 0 : (36 * s) * 1152;
  D.lb = 1024 * t;
  D.xact = (lane >> 4) == t;
  asm volatile("" : "+s"(D.rs_x), "+s"(D.sb_w2), "+s"(D.sb_wo), "+s"(D.sb_x), "+s"(D.lb), "+v"(D.v_w2), "+v"(D.v_wo), "+v"(D.v_x));
  return D;
}


struct Attn { f32x16 S[9]; u32x4 kf[9]; unsigned kl[9]; u32x4 vf[9][2]; float mc; };
DI void attn_load_k(Attn& A, const Params& P, int head, int q0b, int lane) {
  const rsrc_t rk = mk_rsrc(P.ws + WS_K16, 32 * 64 * 1024), rkl = mk_rsrc(P.ws + WS_KL, 32 * 64 * 128);
  const unsigned vkl = (lane >> 5) ? OOB : (lane & 31) * 4;
#pragma unroll
  for (int kt = 0; kt < 9; ++kt) {
    int kb = q0b - 8 + kt; kb = kb < 0 ? 0 : kb;
    A.kf[kt] = __builtin_amdgcn_raw_buffer_load_b128(rk, lane * 16, (head * 64 + kb) * 1024, 16);
    A.kl[kt] = __builtin_amdgcn_raw_buffer_load_b32(rkl, vkl, (head * 64 + kb) * 128, 16);
  }
}
DI void attn_part1(Attn& A, const Params& P, const Carry& C, int head, int q0b, int lane) {
  const int r = lane & 31, h = lane >> 5;
  const rsrc_t rv = mk_rsrc(P.ws + WS_VT, 32 * 64 * 2048);
  u32x4 qf1v; qf1v[0] = C.qf1; qf1v[1] = 0; qf1v[2] = 0; qf1v[3] = 0;
#pragma unroll
  for (int kt = 0; kt < 9; ++kt) {
    f32x16 z = {};
    u32x4 klv; klv[0] = A.kl[kt]; klv[1] = 0; klv[2] = 0; klv[3] = 0;
    z = mfma(A.kf[kt], C.qf0, z);
    A.S[kt] = mfma(klv, qf1v, z);
  }
  SCHED_FENCE;
  const unsigned vvo = (r < 19) ? lane * 16 : OOB;
#pragma unroll
  for (int kt = 0; kt < 9; ++kt) {
    int kb = q0b - 8 + kt; kb = kb < 0 ? 0 : kb;
    A.vf[kt][0] = __builtin_amdgcn_raw_buffer_load_b128(rv, vvo, (head * 64 + kb) * 2048, 16);
    A.vf[kt][1] = __builtin_amdgcn_raw_buffer_load_b128(rv, vvo, (head * 64 + kb) * 2048 + 1024, 16);
  }
  SCHED_FENCE;
  MFMA_SETTLE();
  const float NEG = -1e30f;
#pragma unroll
  for (int i = 0; i < 16; ++i) {
    int row = (i & 3) + 8 * (i >> 2) + 4 * h;
    A.S[0][i] = (row > r) ? A.S[0][i] : NEG;
    A.S[8][i] = (row <= r) ? A.S[8][i] : NEG;
  }
  const int nskip = q0b >= 8 ? 0 : 8 - q0b;
  if (__builtin_expect(nskip > 0, 0)) {
#pragma unroll
    for (int kt = 0; kt < 8; ++kt) {
      if (kt < nskip) {
        asm volatile("; tile before key 0");
#pragma unroll
        for (int i = 0; i < 16; ++i) { float v = NEG; asm volatile("" : "+v"(v)); A.S[kt][i] = v; }
      }
    }
  }
  float mx = NEG;
#pragma unroll
  for (int kt = 0; kt < 9; ++kt)
#pragma unroll
    for (int i = 0; i < 16; ++i) mx = fmaxf(mx, A.S[kt][i]);
  mx = half_max(mx);
  A.mc = -mx * (0.23570226039551584f * 1.4426950408889634f);
}
DI void attn_part2(Attn& A, const Carry& C, int t, int u, int lane) {
  const int r = lane & 31, h = lane >> 5;
  const float csc = 0.23570226039551584f * 1.4426950408889634f, mc = A.mc;
  f32x16 O = {};
#pragma unroll
  for (int kt = 0; kt < 9; ++kt) {
    u32x4 p0, p1;
#pragma unroll
    for (int j = 0; j < 4; ++j) {
      p0[j] = pack2(__builtin_amdgcn_exp2f(__builtin_fmaf(A.S[kt][2 * j], csc, mc)), __builtin_amdgcn_exp2f(__builtin_fmaf(A.S[kt][2 * j + 1], csc, mc)));
      p1[j] = pack2(__builtin_amdgcn_exp2f(__builtin_fmaf(A.S[kt][8 + 2 * j], csc, mc)), __builtin_amdgcn_exp2f(__builtin_fmaf(A.S[kt][8 + 2 * j + 1], csc, mc)));
    }
    O = mfma(A.vf[kt][0], p0, O);
    O = mfma(A.vf[kt][1], p1, O);
  }
  SCHED_FENCE;
  MFMA_SETTLE();
  const float sc = C.gate * __builtin_amdgcn_rcpf(lo_bcast(O[10]));
  u32x4 of; of[0] = pack2(O[0] * sc, O[1] * sc); of[1] = pack2(O[2] * sc, O[3] * sc); of[2] = pack2(O[4] * sc, O[5] * sc); of[3] = pack2(O[6] * sc, O[7] * sc);
  lds_wr128(L_OX + (t * 2 + u) * 1024 + lane * 16, of);
  if (h == 0) lds_wr32(L_OL + (t * 2 + u) * 128 + r * 4, pack2(O[8] * sc, O[9] * sc));
}

template <int U>
DI void front(const Params& P, const int w, const int b, const int m, const int s, u64& ep1, Carry& C) {
  constexpr int u = U;
  const int t = w & 3;
  const int tok0 = 128 * m;
  const int head = 2 * s + u;
  const i32x4 rs_w1 = make_srd(P.w1, DM * FF * 2);
  u32x4 xf[36];
  f32x16 aQK = {}, aVL = {};
  float sxa = 0.f, sxb = 0.f;
  unsigned g1_sb = t * 442368 + 576 * s, g1_lb = 6144 * t;
  SPIN_S(g1_sb); SPIN_S(g1_lb);

  {
    LANEVARS
    const int tid = w * 64 + lane;
    const i32x4 rs_x = make_srd(P.x, SEQ * DM * 2);
    const i32x4 rs_wqkv = make_srd(P.wqkv, DM * 3 * DM * 2);
    unsigned gwa, gwb, b1v; u64 epoch;
    {
      const unsigned short* g0 = P.gw + 64 * w + lane;
      const unsigned short* g1 = P.gw + 512 + lane;
      const unsigned short* pb1 = P.b1 + 288 * s + (tid < 288 ? tid : 0);
      const char* pe = P.ws + WS_EPOCH;
      asm volatile("global_load_ushort %0, %1, off" : "=v"(gwa) : "v"(g0));
      asm volatile("global_load_ushort %0, %1, off" : "=v"(gwb) : "v"(g1));
      asm volatile("global_load_ushort %0, %1, off" : "=v"(b1v) : "v"(pb1));
      asm volatile("global_load_dwordx2 %0, %1, off sc1" : "=v"(epoch) : "v"(pe));
    }
    unsigned vx0, vx1;
    {
      const int rowi = lane >> 3;
      vx0 = rowi * 1152 + (((lane & 7) ^ ((rowi >> 1) & 7)) << 4);
      vx1 = rowi * 1152 + (((lane & 7) ^ ((4 + (rowi >> 1)) & 7)) << 4);
    }
    unsigned vw; bool whole;
    {
      int q = lane >> 4, sg = ((lane & 15) - 4 * q) & 15;
      int mat = sg < 5 ? 0 : (sg < 11 ? 1 : 2);
      int ch = sg < 5 ? sg : (sg < 11 ? sg - 6 : sg - 11);
      whole = (sg == 5);
      vw = q * 3456 + mat * 1152 + ch * 16;
    }
    unsigned x_sb = (tok0 + 16 * w) * 1152, w_sb = 8 * w * 3456 + 72 * s, q_lb = 2048 * w;
    SPIN_S(x_sb); SPIN_S(w_sb); SPIN_S(q_lb);
    auto issue_qkvk = [&](auto pc, auto kc) {
      constexpr int p = decltype(pc)::value, k = decltype(kc)::value;
      constexpr unsigned base = (p & 3) * L_QKV_STAGE;
      if constexpr (k == 0) dma16i<base, p * 128>(rs_x, vx0, x_sb, q_lb);
      else if constexpr (k == 1) dma16i<base + 1024, 8 * 1152 + p * 128>(rs_x, vx1, x_sb, q_lb);
      else { if (!whole) dma16i<base + 16384 + (k - 2) * 1024, (64 * p + 4 * (k - 2)) * 3456>(rs_wqkv, vw, w_sb, q_lb); }
    };
    auto write_gx = [&](int p, unsigned gv) {
      unsigned a = (p & 3) * L_QKV_STAGE + 16384 + (lane >> 2) * 1024 + (lane & 3) * 256 + ((80 + 64 * (lane & 3)) & 255);
      u32x2 v; v[0] = (gv & 0xffffu) | 0x3F800000u; v[1] = 0;
      lds_wr64(a, v);
    };
    static_for<0, 4>([&](auto kc) { issue_qkvk(IC<0>{}, kc); });
    __builtin_amdgcn_s_barrier();
    static_for<0, 4>([&](auto kc) { issue_qkvk(IC<1>{}, kc); });
    const int s1c = tid % 36, s1r = tid / 36;
    u32x4 s1a, s1b, s1d;
    {
      const unsigned so = 36 * m * 9216 + 576 * s;
      const unsigned v0 = (tid < 504) ? s1r * 9216 + s1c * 16 : OOB;
      const unsigned v1 = (tid < 504) ? v0 + 14 * 9216 : OOB;
      const unsigned v2 = (tid < 288) ? v0 + 28 * 9216 : OOB;
      asm volatile("buffer_load_dwordx4 %0, %1, %2, %3 offen" : "=v"(s1a) : "v"(v0), "s"(rs_w1), "s"(so));
      asm volatile("buffer_load_dwordx4 %0, %1, %2, %3 offen" : "=v"(s1b) : "v"(v1), "s"(rs_w1), "s"(so));
      asm volatile("buffer_load_dwordx4 %0, %1, %2, %3 offen" : "=v"(s1d) : "v"(v2), "s"(rs_w1), "s"(so));
    }
    asm volatile("s_waitcnt vmcnt(11)" : "+v"(gwa), "+v"(gwb), "+v"(b1v), "+v"(epoch));
    if (w < 4) write_gx(w, gwa);
    if (tid == 0) lds_wr32(L_GO + 8, 0u);
    if (tid < 288) { u32x4 tv; tv[0] = 0; tv[1] = b1v << 16; tv[2] = b1v & 0xffffu; tv[3] = 0; lds_wr128(L_T + 16 * tid, tv); }
    ep1 = ((u64)(unsigned)__builtin_amdgcn_readfirstlane((int)(unsigned)epoch) | ((u64)(unsigned)__builtin_amdgcn_readfirstlane((int)(unsigned)(epoch >> 32)) << 32)) + 1;
    const unsigned gsel = (w == 0) ? gwb : gwa;
    const unsigned vg1 = ((lane & 31) >> 2) * 9216 + (lane >> 5) * 64 + (lane & 3) * 16;

    const unsigned ax0 = ((32 * t + r) >> 3) * 1024 + (r & 7) * 128 + ((h ^ ((r >> 1) & 7)) << 4);
    unsigned aqkl, avll, aqkh, avlh;
    {
      int lo1 = (blk ? 96 : 0) + (u ? 40 : 0) + 8 * p4;
      int lo2 = blk ? (p4 == 0 ? 32 : (p4 == 1 ? 128 : (p4 == 2 ? 208 : 80))) : 176 + (u ? 40 : 0) + 8 * p4;
      aqkl = 16384 + h * 2048 + q4 * 256 + ((lo1 + 64 * q4) & 255);
      avll = 16384 + h * 2048 + q4 * 256 + ((lo2 + 64 * q4) & 255);
      aqkh = aqkl + 65536; avlh = avll + 65536;
    }
    unsigned axs[4][2];
#pragma unroll
    for (int s4 = 0; s4 < 4; ++s4) { axs[s4][0] = ax0 ^ (s4 << 5); axs[s4][1] = axs[s4][0] + 65536; asm volatile("" : "+v"(axs[s4][0]), "+v"(axs[s4][1])); }
    asm volatile("" : "+v"(aqkl), "+v"(aqkh), "+v"(avll), "+v"(avlh));
    u32x4 fa1[3], fa2[3];
    auto qkv_reads = [&](int j) {
      const int p = j >> 2, s4 = j & 3;
      const int hi = (p >> 1) & 1;
      const unsigned base = (p & 1) * L_QKV_STAGE;
      xf[j] = lds_rd128(axs[s4][hi] + base);
      const unsigned a1 = (hi ? aqkh : aqkl) + base + s4 * 4096, a2 = (hi ? avlh : avll) + base + s4 * 4096;
      fa1[j % 3] = tr_frag(a1, a1 + 1024);
      fa2[j % 3] = tr_frag(a2, a2 + 1024);
    };
    auto qkv_stage_open = [&](int p) {
      if (p == 0) { WAIT_BAR(7); }
      else if (p == 1) { WAIT_BAR(7); }
      else if (p == 2) { WAIT_BAR(8); }
      else if (p == 3) { WAIT_BAR(5); }
      else if (p <= 5) { if (u == 0) WAIT_VM(9); else WAIT_VM(12); BAR_LGKM(); }
      else if (p == 6) { WAIT_BAR(9); }
      else if (p == 7) { WAIT_BAR(4); }
      else { if (u == 0) WAIT_VM(6); else WAIT_VM(9); BAR_LGKM(); }
      if (p >= 1 && p <= 5) { if (w == ((p + 3) & 7)) write_gx(p + 3, gsel); }
    };
    float s1sum = 0.f;
    qkv_stage_open(0);
    qkv_reads(0); qkv_reads(1);
    SCHED_FENCE;
    static_for<0, 36>([&](auto jc) {
      constexpr int j = decltype(jc)::value;
      if constexpr (j + 2 < 36) {
        if constexpr (((j + 2) & 3) == 0) { qkv_stage_open((j + 2) >> 2); SCHED_FENCE; }
        qkv_reads(j + 2);
      }
      {
        constexpr int po = (j + 2) >> 2, k = (j + 2) & 3;
        if constexpr (j < 2) { issue_qkvk(IC<2>{}, IC<2 * j>{}); issue_qkvk(IC<2>{}, IC<2 * j + 1>{}); }
        if constexpr (po == 1) issue_qkvk(IC<3>{}, IC<k>{});
        if constexpr (po >= 1 && po <= 5) issue_qkvk(IC<po + 3>{}, IC<k>{});
        if constexpr (u == 0 && (po == 7 || po == 8)) {
          constexpr int part = po - 7;
          dma16i<L_G1 + part * 24576 + k * 1024, (192 * part + 8 * k) * 9216>(rs_w1, vg1, g1_sb, g1_lb);
          if constexpr (k < 2) dma16i<L_G1 + part * 24576 + (4 + k) * 1024, (192 * part + 8 * (4 + k)) * 9216>(rs_w1, vg1, g1_sb, g1_lb);
        }
      }
      if constexpr (u == 1 && j == 26) {
      const i32x4 rs_s1 = make_srd(P.ws + WS_S1P + (size_t)s * 16 * 2304, 16 * 2304);
      unsigned sb = 9216 * t, lb = L_GATH + 9216 * t; SPIN_S(sb); SPIN_S(lb);
      const unsigned vl = lane * 16;
      static_for<0, 9>([&](auto kc) { dma16i_sc1<decltype(kc)::value * 1024>(rs_s1, vl, sb, lb); });
    }
      if constexpr (u == 1 && j == 31) {
      const i32x4 rs_w2 = make_srd(P.w2, FF * DM * 2), rs_wo = make_srd(P.wo, DM * DM * 2);
      const int idx = t * 64 + lane;
      touch4(rs_w2, idx * 128, (288 * s + 36 * (m >> 1)) * 1152);
      touch4(rs_w2, idx < 68 ? (256 + idx) * 128 : OOB, (288 * s + 36 * (m >> 1)) * 1152);
      touch4(rs_wo, idx < 41 ? idx * 128 : OOB, 36 * s * 1152 + (m >> 1) * 5184);
    }
      if constexpr (u == 1 && j == 10) {
        const unsigned vo = ((lane & 7) < 5) ? (lane >> 3) * 9216 + (lane & 7) * 128 : OOB;
        const unsigned so = (72 * (m >> 1) + 18 * t) * 9216 + 576 * s;
        touch4(rs_w1, vo, so); touch4(rs_w1, vo, so + 8 * 9216); touch4(rs_w1, lane < 16 ? vo : OOB, so + 16 * 9216);
      }
      if constexpr (j == 7) {
        asm volatile("" : "+v"(s1a), "+v"(s1b), "+v"(s1d));
        f32x4 lo, hi;
#pragma unroll
        for (int e = 0; e < 4; ++e) {
          const unsigned a = s1a[e], bb = s1b[e], d = s1d[e];
          const float x0 = bf_lo(a) + bf_lo(bb) + bf_lo(d), x1 = bf_hi(a) + bf_hi(bb) + bf_hi(d);
          if (e < 2) { lo[2 * e] = x0; lo[2 * e + 1] = x1; } else { hi[2 * e - 4] = x0; hi[2 * e - 3] = x1; }
        }
        if (tid < 504) { lds_wrf4(L_S1SCR + (s1r * 288 + 8 * s1c) * 4, lo); lds_wrf4(L_S1SCR + (s1r * 288 + 8 * s1c) * 4 + 16, hi); }
      }
      if constexpr (j == 11) {
        const int c = tid < 288 ? tid : 0;
#pragma unroll
        for (int k = 0; k < 14; ++k) s1sum += lds_rdf(L_S1SCR + (k * 288 + c) * 4);
        PIN(s1sum);
      }
      if constexpr (j == 13) {
        const rsrc_t rs = mk_rsrc(P.ws + WS_S1P + (size_t)(s * 16 + m) * 2304, 2304);
        u32x2 gv; gv[0] = f2u(s1sum); gv[1] = (unsigned)ep1;
        __builtin_amdgcn_raw_buffer_store_b64(gv, rs, tid < 288 ? tid * 8 : OOB, 0, 16);
      }
      SCHED_FENCE;
      aQK = mfma(fa1[j % 3], xf[j], aQK);
      aVL = mfma(fa2[j % 3], xf[j], aVL);
      if constexpr (u == 1) {
#pragma unroll
        for (int e4 = 0; e4 < 4; ++e4) {
          const unsigned e = xf[j][e4];
          const float l = bf_lo(e), hh = bf_hi(e);
          sxa = __builtin_fmaf(l, l, sxa); sxb = __builtin_fmaf(hh, hh, sxb);
        }
        asm volatile("" : "+v"(sxa), "+v"(sxb));
      }
      SCHED_FENCE;
    });
    MFMA_SETTLE();
  }

  {
    LANEVARS
    const unsigned vg1 = ((lane & 31) >> 2) * 9216 + (lane >> 5) * 64 + (lane & 3) * 16;
    const unsigned vg8 = (lane >> 2) * 9216 + (lane & 3) * 16;
    unsigned g8_sb = t * 64 * 9216 + 576 * s + 512, g8_lb = 4096 * t, g8x_sb = (16 + (t >> 1)) * 16 * 9216 + 576 * s + 512, g8x_lb = (16 + (t >> 1)) * 1024;
    SPIN_S(g8_sb); SPIN_S(g8_lb); SPIN_S(g8x_sb); SPIN_S(g8x_lb);
    auto issue_g1 = [&](auto ic, auto pc, auto kc) {
      constexpr int i = decltype(ic)::value, part = decltype(pc)::value, k = decltype(kc)::value;
      dma16i<L_G1 + part * 24576 + k * 1024, (192 * part + 8 * k) * 9216 + 128 * i>(rs_w1, vg1, g1_sb, g1_lb);
    };
    auto issue_g8 = [&](auto hc_, auto kc) {
      constexpr int hc = decltype(hc_)::value, k = decltype(kc)::value;
      if constexpr (k < 4) dma16i<L_G1 + hc * 24576 + k * 1024, (288 * hc + 16 * k) * 9216>(rs_w1, vg8, g8_sb, g8_lb);
      else { if (h == (t & 1)) dma16i<L_G1 + hc * 24576, 288 * hc * 9216>(rs_w1, vg8, g8x_sb, g8x_lb); }
    };
    unsigned ag1a = L_G1 + h * 1024 + u * 512 + q4 * 64 + blk * 32 + p4 * 8;
    unsigned ag1b = ag1a + 49152;
    unsigned ag8 = L_G1 + h * 512 + q4 * 64 + blk * 32 + p4 * 8;
    unsigned ahs = L_H + t * 2048 + lane * 16, ab1 = L_T + r * 16;
    asm volatile("" : "+v"(ag1a), "+v"(ag1b), "+v"(ag8), "+v"(ahs), "+v"(ab1));
    u32x4 fr[4];
    f32x16 acc, accp;
    float gt[16], gy[16];
    u32x4 hq0, hq1, lnf;
    float gK1, gK2;
    auto g1_read = [&](int j) {
      const unsigned a = (j < 24 ? ag1a + (j / 12) * 24576 : ag1b) + (j % 12) * 2048;
      fr[j & 3] = tr_frag(a, a + 256);
    };
    auto gelu_slot = [&](int sl, int fb) {
      if (sl >= 1 && ((sl - 1) & 1) == 0 && (sl - 1) / 2 < 16) { const int e = (sl - 1) / 2;
        const float a2 = accp[e] * accp[e]; gt[e] = __builtin_fmaf(a2, gK2, gK1); PIN(gt[e]); }
      if (sl >= 2 && ((sl - 2) & 1) == 0 && (sl - 2) / 2 < 16) { const int e = (sl - 2) / 2;
        gt[e] = __builtin_amdgcn_exp2f(accp[e] * gt[e]); PIN(gt[e]); }
      if (sl >= 3 && ((sl - 3) & 1) == 0 && (sl - 3) / 2 < 16) { const int e = (sl - 3) / 2;
        gt[e] = __builtin_amdgcn_rcpf(1.0f + gt[e]); PIN(gt[e]); }
      if (sl >= 4 && ((sl - 4) & 1) == 0 && (sl - 4) / 2 < 16) { const int e = (sl - 4) / 2;
        gy[e] = accp[e] * gt[e];
        if (e & 1) { unsigned pk = pack2(gy[e - 1], gy[e]); PIN(pk); if (e < 8) hq0[e >> 1] = pk; else hq1[(e - 8) >> 1] = pk; } else PIN(gy[e]); }
      if (sl == 35) { lds_wr128(ahs + fb * 8192, hq0); lds_wr128(ahs + fb * 8192 + 1024, hq1); }
    };
    auto ln_step = [&](const f32x16& a, int fb) {
      const u32x4 tf = lds_rd128(ab1 + fb * 512);
      return mfma(tf, lnf, a);
    };
    auto gelu_all = [&](const f32x16& a, int fb) {
      u32x4 f0, f1; float y[16];
#pragma unroll
      for (int e = 0; e < 16; ++e) {
        const float x = a[e];
        const float ex = __builtin_amdgcn_exp2f(x * __builtin_fmaf(x * x, gK2, gK1));
        y[e] = x * __builtin_amdgcn_rcpf(1.0f + ex);
      }
#pragma unroll
      for (int j = 0; j < 4; ++j) { f0[j] = pack2(y[2 * j], y[2 * j + 1]); f1[j] = pack2(y[8 + 2 * j], y[8 + 2 * j + 1]); }
      lds_wr128(ahs + fb * 8192, f0); lds_wr128(ahs + fb * 8192 + 1024, f1);
    };
    auto t_write = [&](int col, float v) {
      const unsigned hi = pack2(v, 0.f) & 0xffffu;
      const unsigned lo = pack2(v - bf_lo(hi), 0.f) & 0xffffu;
      lds_wr32(L_T + 16 * col, hi | (hi << 16));
      lds_wr16(L_T + 16 * col + 4, lo);
    };
#define G1_BAR(N) do { if constexpr (u == 0) { WAIT_BAR(N); } else { BAR_LGKM(); } SCHED_FENCE; } while (0)

    float ln_mean = 0.f;
    u32x4 e_kf, e_vt0, e_vt1; unsigned e_lf;
    const unsigned vs = L_VSCR + w * 1280;
    auto qkv_epi = [&](int sl) {
      if (sl == 0) {
        u32x4 vm; vm[0] = pack2(aVL[0], aVL[1]); vm[1] = pack2(aVL[2], aVL[3]); vm[2] = pack2(aVL[4], aVL[5]); vm[3] = pack2(aVL[6], aVL[7]);
        const unsigned vl = u ? pack2(aVL[14], aVL[15]) : pack2(aVL[12], aVL[13]);
        lds_wr128(vs + r * 32 + h * 16, vm);
        if (h == 0) { u32x2 v2; v2[0] = vl; v2[1] = 0x00003F80u; lds_wr64(vs + 1024 + r * 8, v2); }
      }
      if (sl == 6) {
        C.qf0[0] = pack2(aQK[0], aQK[1]); C.qf0[1] = pack2(aQK[2], aQK[3]); C.qf0[2] = pack2(aQK[4], aQK[5]); C.qf0[3] = pack2(aQK[6], aQK[7]);
        asm volatile("" : "+v"(C.qf0));
      }
      if (sl == 11) {
        e_kf[0] = pack2(aQK[8], aQK[9]); e_kf[1] = pack2(aQK[10], aQK[11]); e_kf[2] = pack2(aQK[12], aQK[13]); e_kf[3] = pack2(aQK[14], aQK[15]);
        e_lf = u ? pack2(aVL[10], aVL[11]) : pack2(aVL[8], aVL[9]);
        C.qf1 = h ? 0u : e_lf;
        asm volatile("" : "+v"(e_kf), "+v"(e_lf), "+v"(C.qf1));
      }
      if (sl == 3) {
        asm volatile("" ::: "memory");
        const int k0 = 4 * h + q4;
        const unsigned a0 = blk ? vs + 1024 + k0 * 8 : vs + k0 * 32 + p4 * 8, st = blk ? 64 : 256;
        e_vt0 = tr_frag(a0, a0 + st);
        e_vt1 = tr_frag(a0 + 2 * st, a0 + 3 * st);
      }
      if (sl == 14) {
        const float lg = hi_bcast(aVL[12]), sx = hi_bcast(aVL[13]);
        C.gate = __builtin_amdgcn_rcpf(1.0f + __builtin_amdgcn_exp2f(-1.4426950409f * lg));
        C.omg = __builtin_amdgcn_rcpf(1.0f + __builtin_amdgcn_exp2f(1.4426950409f * lg));
        ln_mean = sx * (1.0f / 576.0f);
        asm volatile("" : "+v"(C.gate), "+v"(C.omg), "+v"(ln_mean));
      }
      if (u == 1 && sl == 16) { const float sxx = half_sum(sxa + sxb); if (h == 0) lds_wr32(L_SXX + (32 * t + r) * 4, f2u(sxx)); }
      if (sl == 20) {
        const size_t hb = (size_t)head * 64 + 4 * m + t;
        const rsrc_t rk = mk_rsrc(P.ws + WS_K16 + hb * 1024, 1024), rkl = mk_rsrc(P.ws + WS_KL + hb * 128, 128), rv = mk_rsrc(P.ws + WS_VT + hb * 2048, 2048);
        __builtin_amdgcn_raw_buffer_store_b128(e_kf, rk, lane * 16, 0, 16);
        __builtin_amdgcn_raw_buffer_store_b32(e_lf, rkl, h ? r * 4 : OOB, 0, 16);
        __builtin_amdgcn_raw_buffer_store_b128(e_vt0, rv, lane * 16, 0, 16);
        __builtin_amdgcn_raw_buffer_store_b128(e_vt1, rv, lane * 16 + 1024, 0, 16);
      }
    };
    G1_BAR(6);
    auto s1_column = [&](int col) {
      const unsigned tag = (unsigned)ep1;
      float sum = 0.f; bool ok = true;
#pragma unroll
      for (int k = 0; k < 16; ++k) { const u32x2 g = *(LAS u32x2*)((LAS char*)smem + L_GATH + k * 2304 + col * 8); const unsigned v0 = g[0], v1 = g[1]; sum += u2f(v0); ok = ok && (v1 == tag); }
      if (!__all(ok)) {
        unsigned spins = 0;
        while (true) {
          sum = 0.f; ok = true;
#pragma unroll 4
          for (int k = 0; k < 16; ++k) { const u64 g = ld_flag(P.ws + WS_S1P + ((size_t)(s * 16 + k) * 288 + col) * 8); sum += u2f((unsigned)g); ok = ok && ((unsigned)(g >> 32) == tag); }
          if (__all(ok) || ++spins > SPIN_CAP) break;
          __builtin_amdgcn_s_sleep(1);
        }
      }
      return sum;
    };
    acc = f32x16{};
    g1_read(0); g1_read(1); g1_read(2);
    SCHED_FENCE;
    static_for<0, 36>([&](auto sc) {
      constexpr int sl = decltype(sc)::value;
      if constexpr (sl == 10) {
        if constexpr (u == 1) WAIT_VM(3);
        G1_BAR(6);
      }
      if constexpr (sl == 22) {
        G1_BAR(10);
        const float sxx = lds_rdf(L_SXX + (32 * t + r) * 4);
        const float var = fmaxf(sxx * (1.0f / 576.0f) - ln_mean * ln_mean, 0.0f);
        const float rstd = __builtin_amdgcn_rsqf(var + 1e-5f);
        const float sd = (var + 1e-5f) * rstd, nm = -ln_mean;
        const unsigned mh = pack2(nm, 0.f) & 0xffffu, ml = pack2(nm - bf_lo(mh), 0.f) & 0xffffu;
        const unsigned sh = pack2(sd, 0.f) & 0xffffu, sl2 = pack2(sd - bf_lo(sh), 0.f) & 0xffffu;
        lnf[0] = h ? 0u : (mh | (ml << 16)); lnf[1] = h ? 0u : (mh | (sh << 16)); lnf[2] = h ? 0u : sl2; lnf[3] = 0u;
        gK1 = -2.3022081986f * rstd; gK2 = -0.1029432397f * rstd * rstd * rstd;
        C.omr = C.omg * rstd;
        asm volatile("" : "+v"(lnf), "+v"(gK1), "+v"(gK2), "+v"(C.omr));
        SCHED_FENCE; }
      if constexpr (u == 1 && sl == 12) { const float v = s1_column(t * 64 + lane); t_write(t * 64 + lane, v); }
      if constexpr (u == 1 && sl == 24) { if (t == 0) { const float v = s1_column(256 + (lane & 31)); if (lane < 32) t_write(256 + lane, v); } }
      if constexpr (sl == 34) G1_BAR(10);
      if constexpr (u == 0) {
        if constexpr (sl < 9 && (sl % 3) != 2) issue_g1(IC<0>{}, IC<2>{}, IC<(sl / 3) * 2 + (sl % 3)>{});
        if constexpr (sl >= 9 && sl < 21 && !(sl & 1)) issue_g1(IC<1>{}, IC<0>{}, IC<(sl - 10) / 2>{});
        if constexpr (sl >= 21 && sl < 33 && !(sl & 1)) issue_g1(IC<1>{}, IC<1>{}, IC<(sl - 22) / 2>{});
        if constexpr (sl == 34) issue_g1(IC<1>{}, IC<2>{}, IC<0>{});
      }
      if constexpr (sl == 10 || sl == 22 || sl == 34) g1_read((sl + 2) % 36);
      if constexpr (sl != 9 && sl != 21 && sl != 33) g1_read((sl + 3) % 36);
      qkv_epi(sl);
      SCHED_FENCE;
      acc = mfma(fr[sl & 3], xf[sl], acc);
      SCHED_FENCE;
    });
    u64 kvf;
    const char* kvfp = P.ws + WS_KVFLAG + (size_t)(((lane & 3) == 0 ? (m >= 2 ? m - 2 : m) : ((lane & 3) == 1 ? (m >= 1 ? m - 1 : m) : m)) * 16 + s) * 128;
    static_for<1, 4>([&](auto ic) {
      constexpr int i = decltype(ic)::value;
      constexpr int fbp = 2 * (i - 1) + u;
      accp = ln_step(acc, fbp);
      acc = f32x16{};
      SCHED_FENCE;
      static_for<0, 36>([&](auto sc) {
        constexpr int sl = decltype(sc)::value;
        if constexpr (sl == 10) G1_BAR(6);
        if constexpr (sl == 22) {
          if constexpr (u == 1 && i == 1) WAIT_VM(0);
          if constexpr (i < 3) G1_BAR(6); else G1_BAR(5);
          if constexpr (i == 1) { if (w == 0 && lane == 0) st_flag(P.ws + WS_KVFLAG + (size_t)(m * 16 + s) * 128, ep1); SCHED_FENCE; }
        }
        if constexpr (sl == 31 && i == 3) asm volatile("global_load_dwordx2 %0, %1, off sc1" : "=v"(kvf) : "v"(kvfp));
        if constexpr (sl == 34) {
          if constexpr (i < 3) G1_BAR(6);
          else {
            if constexpr (u == 1) WAIT_VM(0);
            G1_BAR(0);
            asm volatile("" : "+v"(kvf));
            if (!__all(kvf == ep1)) {
              unsigned spins = 0;
              while (true) { if (__all(ld_flag(kvfp) == ep1) || ++spins > SPIN_CAP) break; __builtin_amdgcn_s_sleep(1); }
            }
            asm volatile("" ::: "memory");
            SCHED_FENCE;
          }
        }
        if constexpr (u == 0) {
          if constexpr (sl < 9 && !(sl & 1)) issue_g1(IC<i>{}, IC<2>{}, IC<1 + sl / 2>{});
          if constexpr (sl >= 9 && sl < 21 && !(sl & 1)) { if constexpr (i < 3) issue_g1(IC<i + 1>{}, IC<0>{}, IC<(sl - 10) / 2>{}); else if constexpr (sl < 20) issue_g8(IC<0>{}, IC<(sl - 10) / 2>{}); }
          if constexpr (sl >= 21 && sl < 33 && !(sl & 1)) { if constexpr (i < 3) issue_g1(IC<i + 1>{}, IC<1>{}, IC<(sl - 22) / 2>{}); else if constexpr (sl < 32) issue_g8(IC<1>{}, IC<(sl - 22) / 2>{}); }
          if constexpr (sl == 34 && i < 3) issue_g1(IC<i + 1>{}, IC<2>{}, IC<0>{});
        }
        static_for<2, 4>([&](auto dc) {
          constexpr int d = decltype(dc)::value, j = sl + d;
          constexpr bool bslot = (sl == 10 || sl == 22 || sl == 34), pre = (sl == 9 || sl == 21 || sl == 33);
          if constexpr ((d == 3 && !pre) || (d == 2 && bslot)) {
            if constexpr (j < 36) g1_read(j);
            else if constexpr (i < 3 || u == 0) {
              const unsigned a = (i < 3) ? (ag1a + (j - 36) * 2048) : (ag8 + (j - 36) * 1024);
              fr[j & 3] = tr_frag(a, a + 256);
            }
          }
        });
        gelu_slot(sl, fbp);
        SCHED_FENCE;
        acc = mfma(fr[sl & 3], xf[sl], acc);
        SCHED_FENCE;
      });
    });
    accp = ln_step(acc, 6 + u);
    Attn A;
    const int q0b = 4 * m + t;
    if constexpr (u == 0) {
      acc = f32x16{};
      SCHED_FENCE;
      static_for<0, 36>([&](auto sc) {
        constexpr int sl = decltype(sc)::value;
        if constexpr (sl + 3 < 36) {
          constexpr int j = sl + 3;
          const unsigned a = ag8 + (j / 18) * 24576 + (j % 18) * 1024;
          fr[j & 3] = tr_frag(a, a + 256);
        }
        if constexpr (sl == 20) attn_load_k(A, P, head, q0b, lane);
        gelu_slot(sl, 6);
        SCHED_FENCE;
        acc = mfma(fr[sl & 3], xf[sl], acc);
        SCHED_FENCE;
      });
      asm volatile("s_waitcnt lgkmcnt(0)" ::: "memory");
      if (lane == 0) lds_add32(L_GO + 8, 1u);
      { unsigned spins = 0; while (__builtin_amdgcn_readfirstlane(lds_ld32v(L_GO + 8)) < 4 && ++spins < SPIN_CAP) __builtin_amdgcn_s_sleep(0); }
      asm volatile("" ::: "memory");
      const G2Dma D = make_g2dma(P, t, s, lane);
      static_for<0, 11>([&](auto kc) { issue_g2<0, L_G2A, decltype(kc)::value>(D); });
      acc = ln_step(acc, 8);
      MFMA_SETTLE();
      gelu_all(acc, 8);
      attn_part1(A, P, C, head, q0b, lane);
      attn_part2(A, C, t, u, lane);
    } else {
      attn_load_k(A, P, head, q0b, lane);
      MFMA_SETTLE();
      gelu_all(accp, 7);
      attn_part1(A, P, C, head, q0b, lane);
      attn_part2(A, C, t, u, lane);
    }
  }
}

struct RedUnit { u32x4 v[4]; };
DI void red_load(RedUnit& R, const Params& P, int m, int s, int i, int q, int lane, bool valid = true) {
  const rsrc_t rp = mk_rsrc(P.ws + WS_PART + (size_t)m * 16 * 147456, 16 * 147456);
  const unsigned vo = valid ? (lane >> 4) * 147456 + (16 * q + (lane & 15)) * 16 : OOB;
  const unsigned so = ((2 * i + (s >> 3)) * 4 + ((s >> 1) & 3)) * 2048 + (s & 1) * 1024;
#pragma unroll
  for (int j = 0; j < 4; ++j) R.v[j] = __builtin_amdgcn_raw_buffer_load_b128(rp, vo, so + j * 4 * 147456, 16);
}
DI void red_sum(const RedUnit& R, float (&a)[8], int k) {
  const unsigned x0 = R.v[0][k], x1 = R.v[1][k], x2 = R.v[2][k], x3 = R.v[3][k];
  a[2 * k] = (bf_lo(x0) + bf_lo(x1)) + (bf_lo(x2) + bf_lo(x3));
  a[2 * k + 1] = (bf_hi(x0) + bf_hi(x1)) + (bf_hi(x2) + bf_hi(x3));
}
DI unsigned red_cross(float (&a)[8]) {
  float bq[4];
#pragma unroll
  for (int k = 0; k < 4; ++k) { auto p = __builtin_amdgcn_permlane32_swap(f2u(a[k]), f2u(a[k + 4]), false, false); const unsigned p0 = p[0], p1 = p[1]; bq[k] = u2f(p0) + u2f(p1); }
  auto c0 = __builtin_amdgcn_permlane16_swap(f2u(bq[0]), f2u(bq[2]), false, false);
  auto c1 = __builtin_amdgcn_permlane16_swap(f2u(bq[1]), f2u(bq[3]), false, false);
  const unsigned c00 = c0[0], c01 = c0[1], c10 = c1[0], c11 = c1[1];
  return pack2(u2f(c00) + u2f(c01), u2f(c10) + u2f(c11));
}
DI void red_store(unsigned pk, const Params& P, int m, int s, int i, int q, int lane, bool go) {
  const int rho = lane >> 4, r = 16 * (q & 1) + (lane & 15), h = q >> 1;
  const rsrc_t ro = mk_rsrc(P.out + (size_t)(128 * m + 32 * ((s >> 1) & 3)) * DM + 32 * (2 * i + (s >> 3)) + 16 * (s & 1), go ? 32 * DM * 2 : 0);
  __builtin_amdgcn_raw_buffer_store_b32(pk, ro, (r * DM + 4 * h + 8 * (rho >> 1) + 2 * (rho & 1)) * 2, 0, 0);
}

template <int U>
DI void gemm2(const Params& P, const int w, const int m, const int s, const float omg, const float omr, const u64 ep1) {
  constexpr int u = U;
  const int t = w & 3;
  LANEVARS
  G2Dma D;
  if constexpr (u == 0) D = make_g2dma(P, t, s, lane);
  u32x4 hf[21];
#pragma unroll
  for (int fb = 0; fb < 9; ++fb) {
    hf[2 * fb] = lds_rd128(L_H + (fb * 4 + t) * 2048 + lane * 16);
    hf[2 * fb + 1] = lds_rd128(L_H + (fb * 4 + t) * 2048 + 1024 + lane * 16);
  }
  hf[18] = lds_rd128(L_OX + (t * 2 + 0) * 1024 + lane * 16);
  hf[19] = lds_rd128(L_OX + (t * 2 + 1) * 1024 + lane * 16);
  {
    unsigned la = lds_rd32(L_OL + (t * 2 + 0) * 128 + r * 4), lb = lds_rd32(L_OL + (t * 2 + 1) * 128 + r * 4);
    const unsigned oh = pack2(omg, 0.f) & 0xffffu;
    const unsigned ol = pack2(omg - bf_lo(oh), 0.f) & 0xffffu;
    const unsigned ob = (s == 0) ? (oh | (ol << 16)) : 0u;
    hf[20][0] = h ? ob : la; hf[20][1] = h ? 0u : lb; hf[20][2] = 0; hf[20][3] = 0;
    asm volatile("" : "+v"(hf[20][0]), "+v"(hf[20][1]));
  }
  BAR_LGKM();
  if constexpr (u == 0) {
    static_for<0, 11>([&](auto kc) { issue_g2<1, L_G2B, decltype(kc)::value>(D); });
    issue_g2<2, L_G2C, 0>(D);
  }
  unsigned ag2 = u * 512 + (4 * h + q4) * 64 + blk * 32 + p4 * 8;
  unsigned ag2A = ag2 + L_G2A, ag2B = ag2 + L_G2B, ag2C = ag2 + L_G2C, vst = lane * 16;
  float om = omr;
  asm volatile("" : "+v"(ag2A), "+v"(ag2B), "+v"(ag2C), "+v"(vst), "+v"(om));
  char* const partbase = P.ws + WS_PART + ((size_t)(m * 16 + s) * 18 * 4 + u * 4 + t) * 2048;
  constexpr int PF = 2;
  u32x4 fr[PF + 1];
  f32x16 accH, accA, pH, pA;
  u32x4 oq;
  auto g2_store = [&](int i, int half) {
    const rsrc_t rq = mk_rsrc(partbase + (size_t)i * 8 * 2048, 2048);
    __builtin_amdgcn_raw_buffer_store_b128(oq, rq, vst + half * 1024, 0, 16);
  };
  auto g2_epi = [&](int sl0, int i) {
    const int sl = sl0;
    if (sl >= 3 && sl <= 6) { const int j = sl - 3;
      unsigned pk = pack2(__builtin_fmaf(pH[2 * j], om, pA[2 * j]), __builtin_fmaf(pH[2 * j + 1], om, pA[2 * j + 1])); PIN(pk); oq[j] = pk; }
    if (sl == 7) g2_store(i, 0);
    if (sl >= 8 && sl <= 11) { const int j = sl - 8;
      unsigned pk = pack2(__builtin_fmaf(pH[8 + 2 * j], om, pA[8 + 2 * j]), __builtin_fmaf(pH[8 + 2 * j + 1], om, pA[8 + 2 * j + 1])); PIN(pk); oq[j] = pk; }
    if (sl == 12) g2_store(i, 1);
  };
  u64 pfa = 0, pfb = 0;
  const char* pfp = P.ws + WS_PFLAG + ((size_t)m * 9 + (lane >> 4)) * 128 + (lane & 15) * 8;
  fr[0] = tr_frag(ag2A, ag2A + 1024); fr[1] = tr_frag(ag2A + 2048, ag2A + 3072);
  if constexpr (PF == 3) fr[2] = tr_frag(ag2A + 4096, ag2A + 5120);
  SCHED_FENCE;
  static_for<0, 9>([&](auto ic) {
    constexpr int i = decltype(ic)::value;
    constexpr unsigned prv = ((i + 2) % 3 == 0) ? L_G2A : (((i + 2) % 3 == 1) ? L_G2B : L_G2C);
    constexpr unsigned cur = (i % 3 == 0) ? L_G2A : ((i % 3 == 1) ? L_G2B : L_G2C);
    const unsigned cura = (i % 3 == 0) ? ag2A : ((i % 3 == 1) ? ag2B : ag2C);
    const unsigned nxta = ((i + 1) % 3 == 0) ? ag2A : (((i + 1) % 3 == 1) ? ag2B : ag2C);
    static_for<0, 21>([&](auto sc) {
      constexpr int sl = decltype(sc)::value;
      if constexpr (u == 1 && sl == 20 - PF && i >= 2) WAIT_VM(2);
      if constexpr (u == 1 && i == 8) {
        if constexpr (sl == 4) {
          const i32x4 rs_pf = make_srd(P.ws, (unsigned)WS_END);
          const unsigned pfo = t == 0 ? (unsigned)WS_PFLAG + (unsigned)((m * 9 + (lane >> 4)) * 128 + (lane & 15) * 8) : OOB;
          asm volatile("buffer_load_dwordx2 %0, %1, %2, 0 offen sc1" : "=v"(pfa) : "v"(pfo), "s"(rs_pf));
          asm volatile("buffer_load_dwordx2 %0, %1, %2, 0 offen offset:512 sc1" : "=v"(pfb) : "v"(pfo), "s"(rs_pf)); }
        if constexpr (sl == 20 - PF) { asm volatile("" : "+v"(pfa), "+v"(pfb)); if (t == 0) {
          const u64 ma = __ballot(pfa == ep1), mb = __ballot(pfb == ep1);
          unsigned rdy = 0;
#pragma unroll
          for (int k = 0; k < 4; ++k) rdy |= (((ma >> (16 * k)) & 0xffffu) == 0xffffu ? 1u : 0u) << k;
#pragma unroll
          for (int k = 0; k < 2; ++k) rdy |= (((mb >> (16 * k)) & 0xffffu) == 0xffffu ? 1u : 0u) << (4 + k);
          if (lane == 0) lds_wr32(L_GO, rdy); } }
      }
      if constexpr (sl == 21 - PF) {
        if constexpr (u == 0) { if constexpr (i < 7) { WAIT_BAR(11); } else { WAIT_BAR(2); } } else { BAR_LGKM(); }
        if constexpr (u == 0 && i >= 2) { if (w == 0 && lane == 0) st_flag(P.ws + WS_PFLAG + ((size_t)m * 9 + (i - 2)) * 128 + s * 8, ep1); }
        SCHED_FENCE;
      }
      if constexpr (u == 0) {
        if constexpr (sl == 21 - PF && i + 3 < 9) issue_g2<i + 3, cur, 0>(D);
        if constexpr (i + 2 < 9 && ((sl < 18 && !(sl & 1)) || sl == 17)) issue_g2<i + 2, prv, (sl == 17 ? 10 : 1 + sl / 2)>(D);
      }
      constexpr int rot = (i * 21) % (PF + 1);
      if constexpr (sl + PF < 21) {
        constexpr int j = sl + PF;
        const unsigned a = cura + (j < 20 ? j * 2048 : 40 * 1024);
        fr[(rot + j) % (PF + 1)] = tr_frag(a, j < 20 ? a + 1024 : a);
      } else if constexpr (i < 8) {
        constexpr int j = sl + PF - 21;
        const unsigned a = nxta + j * 2048;
        fr[(rot + 21 + j) % (PF + 1)] = tr_frag(a, a + 1024);
      }
      if constexpr (i > 0) g2_epi(sl, i - 1);
      SCHED_FENCE;
      constexpr int fi = (rot + sl) % (PF + 1);
      if constexpr (sl == 0) { if constexpr (i > 0) { pH = accH; pA = accA; } f32x16 z = {}; accH = mfma(fr[fi], hf[0], z); }
      else if constexpr (sl < 18) accH = mfma(fr[fi], hf[sl], accH);
      else if constexpr (sl == 18) { f32x16 z = {}; accA = mfma(fr[fi], hf[sl], z); }
      else accA = mfma(fr[fi], hf[sl], accA);
      SCHED_FENCE;
    });
  });
  MFMA_SETTLE();
  pH = accH; pA = accA;
#pragma unroll
  for (int sl = 3; sl <= 17; ++sl) g2_epi(sl, 8);
}

__global__ __launch_bounds__(512, 2) void fused_kernel(Params P) {
  {
    u64 pc; unsigned len;
    asm volatile("s_getpc_b64 %0\nfused_code_here:\n\ts_mov_b32 %1, fused_code_end-fused_code_here" : "=s"(pc), "=s"(len));
    i32x4 srd; srd[0] = (int)(unsigned)pc; srd[1] = (int)((unsigned)(pc >> 32) & 0xffffu); srd[2] = (int)len; srd[3] = 0x00020000;
    touch4(srd, threadIdx.x * 128, 0); touch4(srd, threadIdx.x * 128, 65536);
  }
  const int w = __builtin_amdgcn_readfirstlane(threadIdx.x >> 6);
  const int t = w & 3, u = w >> 2;
  const int b = blockIdx.x, xcd = b & 7, slot = b >> 3;
  const int m = 2 * (slot >> 2) + (xcd & 1), s = 4 * (xcd >> 1) + (slot & 3);
  u64 ep1;
  Carry C;
  if (u == 0) front<0>(P, w, b, m, s, ep1, C); else front<1>(P, w, b, m, s, ep1, C);
  WAIT_BAR(0);
  if (u == 0) gemm2<0>(P, w, m, s, C.omg, C.omr, ep1); else gemm2<1>(P, w, m, s, C.omg, C.omr, ep1);
  {
    const int lane = fresh_lane();
    unsigned pend = (w >> 2) ? 0x0aau : 0x155u;
    {
      unsigned todo = pend & (unsigned)__builtin_amdgcn_readfirstlane(lds_rd32(L_GO)) & 0x3fu;
      pend &= ~todo;
      int ui[3]; RedUnit R[3];
#pragma unroll
      for (int k = 0; k < 3; ++k) {
        ui[k] = todo ? __builtin_ctz(todo) : -1;
        todo &= todo - 1;
        red_load(R[k], P, m, s, ui[k] < 0 ? 0 : ui[k], w & 3, lane, ui[k] >= 0);
      }
      WAIT_BAR(12);
      if (w == 0 && lane == 0) st_flag(P.ws + WS_PFLAG + ((size_t)m * 9 + 8) * 128 + s * 8, ep1);
#pragma unroll
      for (int k = 0; k < 3; ++k) {
        float a[8];
#pragma unroll
        for (int j = 0; j < 4; ++j) red_sum(R[k], a, j);
        red_store(red_cross(a), P, m, s, ui[k] < 0 ? 0 : ui[k], w & 3, lane, ui[k] >= 0);
      }
    }
    const char* pfp = P.ws + WS_PFLAG + ((size_t)m * 9 + (lane >> 4)) * 128 + (lane & 15) * 8;
    unsigned spins = 0;
    while (pend) {
      const u64 fa = ld_flag(pfp), fb = ld_flag(pfp + 512), fc = ld_flag(P.ws + WS_PFLAG + ((size_t)m * 9 + 8) * 128 + (lane & 15) * 8);
      const u64 ma = __ballot(fa == ep1), mb = __ballot(fb == ep1);
      unsigned rdy = __all(fc == ep1) ? 0x1ffu : 0u;
#pragma unroll
      for (int k = 0; k < 4; ++k) rdy |= (((ma >> (16 * k)) & 0xffffu) == 0xffffu ? 1u : 0u) << k;
#pragma unroll
      for (int k = 0; k < 3; ++k) rdy |= (((mb >> (16 * k)) & 0xffffu) == 0xffffu ? 1u : 0u) << (4 + k);
      if (++spins > SPIN_CAP) rdy = 0x1ffu;
      unsigned todo = pend & rdy;
      if (!todo) { __builtin_amdgcn_s_sleep(1); continue; }
      asm volatile("" ::: "memory");
      int ui[5]; RedUnit R[5];
      pend &= ~todo;
#pragma unroll
      for (int k = 0; k < 5; ++k) {
        ui[k] = todo ? __builtin_ctz(todo) : -1;
        todo &= todo - 1;
        if (ui[k] >= 0) red_load(R[k], P, m, s, ui[k], w & 3, lane);
      }
#pragma unroll
      for (int k = 0; k < 5; ++k) {
        if (ui[k] >= 0) {
          float a[8];
#pragma unroll
          for (int j = 0; j < 4; ++j) red_sum(R[k], a, j);
          red_store(red_cross(a), P, m, s, ui[k], w & 3, lane, true);
        }
      }
    }
  }
  if (b == 0 && w == 0) {
    const int lane = fresh_lane();
    unsigned spins = 0;
    while (true) {
      bool ok = true;
#pragma unroll
      for (int k = 0; k < 4; ++k) ok &= (ld_flag(P.ws + WS_KVFLAG + (size_t)(lane * 4 + k) * 128) == ep1);
      if (__all(ok)) break;
      if (++spins > SPIN_CAP) break;
      __builtin_amdgcn_s_sleep(2);
    }
    if (lane == 0) st_flag(P.ws + WS_EPOCH, ep1);
  }
  asm volatile("fused_code_end:");
}

extern "C" void kernel_launch(void* const* d_in, const int* in_sizes, int n_in, void* d_out, int out_size, void* d_ws, size_t ws_size, hipStream_t stream) {
  Params P;
  P.x = (const unsigned short*)d_in[0]; P.wqkv = (const unsigned short*)d_in[1]; P.wo = (const unsigned short*)d_in[2];
  P.w1 = (const unsigned short*)d_in[4]; P.b1 = (const unsigned short*)d_in[5]; P.w2 = (const unsigned short*)d_in[6];
  P.b2 = (const unsigned short*)d_in[7]; P.gw = (const unsigned short*)d_in[8];
  P.ws = (char*)d_ws; P.out = (unsigned short*)d_out;
  fused_kernel<<<256, 512, 0, stream>>>(P);
}
```
